```python
import torch

def _softmax(x, axis=-1):
    return torch.softmax(x, dim=axis)
def _gelu(x):
    return torch.nn.functional.gelu(x, approximate="tanh")
def _silu(x):
    return torch.nn.functional.silu(x)
def _sigmoid(x):
    return torch.sigmoid(x)
def _layernorm(x, axis=-1, eps=1e-5):
    m = x.mean(dim=axis, keepdim=True)
    v = x.var(dim=axis, keepdim=True, unbiased=False)
    return (x - m) / torch.sqrt(v + eps)
def _scatter_add(tgt, idx, src, axis=0):
    return tgt.clone().index_add_(axis, idx, src)
def _segment_sum(src, seg, n):
    out = torch.zeros((n,) + src.shape[1:], dtype=src.dtype)
    return out.index_add_(0, seg, src)
def _pad(x, pw):
    flat = tuple(v for pair in reversed(pw) for v in pair)
    return torch.nn.functional.pad(x, flat)

kernel_name = 'interleave_layers__gqa_block_cached__geglu_ffn__decode'

def setup_inputs():

    _w = (lambda a: (lambda t: t.to(torch.bfloat16) if t.is_floating_point() else t)(torch.as_tensor(a)))
    def _A_setup():
        B, Sq, D = 4, 1, 448
        Hq, Hkv, Sk, Dh = 16, 8, 4096, 256
        assert Hq % Hkv == 0, (Hq, Hkv)
        return {
            "x": _w((lambda *s: torch.randn(*s, dtype=torch.bfloat16) * 0.05)(B, Sq, D)),
            "k_cache": _w((lambda *s: torch.randn(*s, dtype=torch.bfloat16) * 1.6)(B, Hkv, Sk, Dh)),
            "v_cache": _w((lambda *s: torch.randn(*s, dtype=torch.bfloat16) * 1.6)(B, Hkv, Sk, Dh)),
            "wq": _w((lambda *s: torch.randn(*s, dtype=torch.bfloat16) * 1.6)(D, Hq * Dh) * (D ** -0.5)),
            "wo": _w((lambda *s: torch.randn(*s, dtype=torch.bfloat16) * 1.6)(Hq * Dh, D) * ((Hq * Dh) ** -0.5)),
        }
    _A_in = _A_setup()

    def _B_setup():
        B, S, D, F = 4, 1, 448, 1792
        return {
            "x": _w((lambda *s: torch.randn(*s, dtype=torch.bfloat16) * 0.05)(B, S, D)),
            "w_gate": _w((lambda *s: torch.randn(*s, dtype=torch.bfloat16) * 0.05)(D, F)),
            "w_up": _w((lambda *s: torch.randn(*s, dtype=torch.bfloat16) * 0.05)(D, F)),
            "w_down": _w((lambda *s: torch.randn(*s, dtype=torch.bfloat16) * 0.05)(F, D)),
        }
    _B_in = _B_setup()

    return {
        "A_x": _A_in["x"],
        "A_k_cache": _A_in["k_cache"],
        "A_v_cache": _A_in["v_cache"],
        "A_wq": _A_in["wq"],
        "A_wo": _A_in["wo"],
        "B_x": _B_in["x"],
        "B_w_gate": _B_in["w_gate"],
        "B_w_up": _B_in["w_up"],
        "B_w_down": _B_in["w_down"],
    }

def reference(A_x, A_k_cache, A_v_cache, A_wq, A_wo, B_x, B_w_gate, B_w_up, B_w_down):
    def _A_ref(x, k_cache, v_cache, wq, wo):
        B, Sq, D = x.shape
        Hq, Dh = 16, 256
        Hkv = k_cache.shape[1]
        rep = Hq // Hkv
        q = (lambda x, *d: x.permute(*d))((x @ wq).reshape(B, Sq, Hq, Dh), 0, 2, 1, 3)
        k = (lambda x, n, axis: torch.repeat_interleave(x, n, dim=axis))(k_cache, rep, axis=1)
        v = (lambda x, n, axis: torch.repeat_interleave(x, n, dim=axis))(v_cache, rep, axis=1)
        scores = torch.einsum("bhqd,bhkd->bhqk", q, k) * (Dh ** -0.5)
        p = _softmax(scores, axis=-1)
        o = torch.einsum("bhqk,bhkd->bhqd", p, v)
        return (lambda x, *d: x.permute(*d))(o, 0, 2, 1, 3).reshape(B, Sq, Hq * Dh) @ wo

    def _B_ref(x, w_gate, w_up, w_down):
        return (_gelu(x @ w_gate) * (x @ w_up)) @ w_down

    x = A_x
    x = x + _A_ref(_layernorm(x, axis=-1), A_k_cache, A_v_cache, A_wq, A_wo)
    x = x + _B_ref(_layernorm(x, axis=-1), B_w_gate, B_w_up, B_w_down)
    x = x + _A_ref(_layernorm(x, axis=-1), A_k_cache, A_v_cache, A_wq, A_wo)
    x = x + _B_ref(_layernorm(x, axis=-1), B_w_gate, B_w_up, B_w_down)
    return x
```

```cpp
#include <hip/hip_runtime.h>
#include <hip/hip_bf16.h>
#include <stdint.h>

constexpr int B = 4, D = 448, HQ = 16, HKV = 8, SK = 4096, DH = 256, F = 1792;
constexpr int NSPLIT = 16;
typedef unsigned short u16;
__device__ __forceinline__ float bf2f(u16 v) { return __uint_as_float(((unsigned)v) << 16); }
__device__ __forceinline__ u16 f2bf(float f) { __hip_bfloat16 h = __float2bfloat16(f); return *(u16*)&h; }
__device__ __forceinline__ float rbf(float f) { return bf2f(f2bf(f)); }

__device__ void ln_rows(const float* x, float* xn  ) {
  int w = threadIdx.x >> 6, l = threadIdx.x & 63;
  float v[7]; float s = 0;
  for (int i = 0; i < 7; i++) { v[i] = x[w * D + l + 64 * i]; s += v[i]; }
  for (int o = 32; o; o >>= 1) s += __shfl_xor(s, o);
  float m = s / D; float q = 0;
  for (int i = 0; i < 7; i++) { float d = v[i] - m; q += d * d; }
  for (int o = 32; o; o >>= 1) q += __shfl_xor(q, o);
  float r = 1.0f / sqrtf(q / D + 1e-5f);
  for (int i = 0; i < 7; i++) xn[w * D + l + 64 * i] = rbf((v[i] - m) * r);
  __syncthreads();
}

__global__ void k_init(const u16* x, float* xf) {
  int i = blockIdx.x * 256 + threadIdx.x;
  if (i < B * D) xf[i] = bf2f(x[i]);
}

__global__ void k_q(const float* x, const u16* wq, float* q) {
  __shared__ float xn[B * D];
  ln_rows(x, xn);
  __shared__ float red[4][4][64];
  int w = threadIdx.x >> 6, l = threadIdx.x & 63;
  int n = blockIdx.x * 64 + l;
  float a[4] = {0, 0, 0, 0};
  for (int k = w * 112; k < w * 112 + 112; k++) {
    float wv = bf2f(wq[k * 4096 + n]);
    for (int b = 0; b < 4; b++) a[b] += xn[b * D + k] * wv;
  }
  for (int b = 0; b < 4; b++) red[w][b][l] = a[b];
  __syncthreads();
  q[w * 4096 + n] = rbf(red[0][w][l] + red[1][w][l] + red[2][w][l] + red[3][w][l]);
}

__global__ void k_attn(const float* q, const u16* kc, const u16* vc, float* pm, float* po) {
  int s = blockIdx.x, h = blockIdx.y, b = blockIdx.z;
  constexpr int KEYS = SK / NSPLIT;
  __shared__ float sc[2][KEYS];
  __shared__ float red[2][8];
  __shared__ float ored[4][2][DH];
  int w = threadIdx.x >> 6, l = threadIdx.x & 63;
  const u16* kp = kc + ((size_t)(b * HKV + h) * SK + s * KEYS) * DH;
  const u16* vp = vc + ((size_t)(b * HKV + h) * SK + s * KEYS) * DH;
  float qv[2][8];
  for (int g = 0; g < 2; g++) for (int i = 0; i < 8; i++) qv[g][i] = q[b * 4096 + (h * 2 + g) * DH + (l & 31) * 8 + i];
  for (int k = w * 2 + (l >> 5); k < KEYS; k += 8) {
    uint4 kv = *(const uint4*)(kp + (size_t)k * DH + (l & 31) * 8);
    unsigned u[4] = {kv.x, kv.y, kv.z, kv.w};
    float a0 = 0, a1 = 0;
    for (int i = 0; i < 4; i++) {
      float lo = __uint_as_float(u[i] << 16), hi = __uint_as_float(u[i] & 0xffff0000u);
      a0 += lo * qv[0][2 * i] + hi * qv[0][2 * i + 1];
      a1 += lo * qv[1][2 * i] + hi * qv[1][2 * i + 1];
    }
    for (int o = 16; o; o >>= 1) { a0 += __shfl_xor(a0, o); a1 += __shfl_xor(a1, o); }
    if ((l & 31) == 0) { sc[0][k] = a0 * 0.0625f; sc[1][k] = a1 * 0.0625f; }
  }
  __syncthreads();
  float m0 = -1e30f, m1 = -1e30f;
  for (int k = threadIdx.x; k < KEYS; k += 256) { m0 = fmaxf(m0, sc[0][k]); m1 = fmaxf(m1, sc[1][k]); }
  for (int o = 32; o; o >>= 1) { m0 = fmaxf(m0, __shfl_xor(m0, o)); m1 = fmaxf(m1, __shfl_xor(m1, o)); }
  if (l == 0) { red[0][w] = m0; red[1][w] = m1; }
  __syncthreads();
  m0 = fmaxf(fmaxf(red[0][0], red[0][1]), fmaxf(red[0][2], red[0][3]));
  m1 = fmaxf(fmaxf(red[1][0], red[1][1]), fmaxf(red[1][2], red[1][3]));
  float l0 = 0, l1 = 0;
  for (int k = threadIdx.x; k < KEYS; k += 256) {
    float p0 = __expf(sc[0][k] - m0), p1 = __expf(sc[1][k] - m1);
    sc[0][k] = p0; sc[1][k] = p1; l0 += p0; l1 += p1;
  }
  for (int o = 32; o; o >>= 1) { l0 += __shfl_xor(l0, o); l1 += __shfl_xor(l1, o); }
  if (l == 0) { red[0][4 + w] = l0; red[1][4 + w] = l1; }
  __syncthreads();
  l0 = red[0][4] + red[0][5] + red[0][6] + red[0][7];
  l1 = red[1][4] + red[1][5] + red[1][6] + red[1][7];
  float o0[8] = {0}, o1[8] = {0};
  for (int k = w * 2 + (l >> 5); k < KEYS; k += 8) {
    uint4 vv = *(const uint4*)(vp + (size_t)k * DH + (l & 31) * 8);
    unsigned u[4] = {vv.x, vv.y, vv.z, vv.w};
    float p0 = sc[0][k], p1 = sc[1][k];
    for (int i = 0; i < 4; i++) {
      float lo = __uint_as_float(u[i] << 16), hi = __uint_as_float(u[i] & 0xffff0000u);
      o0[2 * i] += p0 * lo; o0[2 * i + 1] += p0 * hi;
      o1[2 * i] += p1 * lo; o1[2 * i + 1] += p1 * hi;
    }
  }
  for (int i = 0; i < 8; i++) { o0[i] += __shfl_xor(o0[i], 32); o1[i] += __shfl_xor(o1[i], 32); }
  if (l < 32) for (int i = 0; i < 8; i++) { ored[w][0][l * 8 + i] = o0[i]; ored[w][1][l * 8 + i] = o1[i]; }
  __syncthreads();
  int t = threadIdx.x;
  for (int g = 0; g < 2; g++) {
    size_t idx = ((size_t)(b * HQ + h * 2 + g) * NSPLIT + s);
    po[idx * DH + t] = ored[0][g][t] + ored[1][g][t] + ored[2][g][t] + ored[3][g][t];
    if (t == 0) { pm[idx * 2] = g ? m1 : m0; pm[idx * 2 + 1] = g ? l1 : l0; }
  }
}

__global__ void k_comb(const float* pm, const float* po, float* o) {
  int bh = blockIdx.x, t = threadIdx.x;
  float m = -1e30f;
  for (int s = 0; s < NSPLIT; s++) m = fmaxf(m, pm[(bh * NSPLIT + s) * 2]);
  float l = 0, a = 0;
  for (int s = 0; s < NSPLIT; s++) {
    float e = __expf(pm[(bh * NSPLIT + s) * 2] - m);
    l += e * pm[(bh * NSPLIT + s) * 2 + 1];
    a += e * po[(size_t)(bh * NSPLIT + s) * DH + t];
  }
  o[bh * DH + t] = rbf(a / l);
}

template <int K, bool OUT>
__global__ void k_proj(const float* in, const u16* W, const float* x, float* y, u16* out) {
  __shared__ float red[16][4][16];
  int c = threadIdx.x & 15, g = threadIdx.x >> 4;
  int n = blockIdx.x * 16 + c;
  float a[4] = {0, 0, 0, 0};
  for (int k = g; k < K; k += 16) {
    float wv = bf2f(W[k * D + n]);
    for (int b = 0; b < 4; b++) a[b] += in[b * K + k] * wv;
  }
  for (int b = 0; b < 4; b++) red[g][b][c] = a[b];
  __syncthreads();
  if (threadIdx.x < 64) {
    int b = threadIdx.x >> 4;
    float s = 0;
    for (int i = 0; i < 16; i++) s += red[i][b][c];
    float r = rbf(x[b * D + n] + rbf(s));
    y[b * D + n] = r;
    if (OUT) out[b * D + n] = f2bf(r);
  }
}

__global__ void k_ffn1(const float* x, const u16* wg, const u16* wu, float* h) {
  __shared__ float xn[B * D];
  ln_rows(x, xn);
  __shared__ float red[4][8][64];
  int w = threadIdx.x >> 6, l = threadIdx.x & 63;
  int n = blockIdx.x * 64 + l;
  float a[4] = {0, 0, 0, 0}, u[4] = {0, 0, 0, 0};
  for (int k = w * 112; k < w * 112 + 112; k++) {
    float gv = bf2f(wg[k * F + n]), uv = bf2f(wu[k * F + n]);
    for (int b = 0; b < 4; b++) { a[b] += xn[b * D + k] * gv; u[b] += xn[b * D + k] * uv; }
  }
  for (int b = 0; b < 4; b++) { red[w][b][l] = a[b]; red[w][4 + b][l] = u[b]; }
  __syncthreads();
  float g = rbf(red[0][w][l] + red[1][w][l] + red[2][w][l] + red[3][w][l]);
  float uu = rbf(red[0][4 + w][l] + red[1][4 + w][l] + red[2][4 + w][l] + red[3][4 + w][l]);
  float ge = 0.5f * g * (1.0f + tanhf(0.7978845608028654f * (g + 0.044715f * g * g * g)));
  h[w * F + n] = rbf(rbf(ge) * uu);
}

extern "C" void kernel_launch(void* const* d_in, const int* in_sizes, int n_in, void* d_out, int out_size, void* d_ws,
                              size_t ws_size, hipStream_t stream) {
  const u16* x = (const u16*)d_in[0];
  const u16* kc = (const u16*)d_in[1];
  const u16* vc = (const u16*)d_in[2];
  const u16* wq = (const u16*)d_in[3];
  const u16* wo = (const u16*)d_in[4];
  const u16* wg = (const u16*)d_in[6];
  const u16* wu = (const u16*)d_in[7];
  const u16* wd = (const u16*)d_in[8];
  float* ws = (float*)d_ws;
  float* x0 = ws;
  float* x1 = ws + 2048;
  float* q = ws + 4096;
  float* o = q + 16384;
  float* h = o + 16384;
  float* pm = h + 8192;
  float* po = pm + 4096;
  k_init<<<7, 256, 0, stream>>>(x, x0);
  for (int layer = 0; layer < 2; layer++) {
    k_q<<<64, 256, 0, stream>>>(x0, wq, q);
    k_attn<<<dim3(NSPLIT, HKV, B), 256, 0, stream>>>(q, kc, vc, pm, po);
    k_comb<<<B * HQ, 256, 0, stream>>>(pm, po, o);
    k_proj<4096, false><<<28, 256, 0, stream>>>(o, wo, x0, x1, nullptr);
    k_ffn1<<<28, 256, 0, stream>>>(x1, wg, wu, h);
    if (layer == 0) k_proj<F, false><<<28, 256, 0, stream>>>(h, wd, x1, x0, nullptr);
    else k_proj<F, true><<<28, 256, 0, stream>>>(h, wd, x1, x0, (u16*)d_out);
  }
}
```

```cpp
#include <hip/hip_runtime.h>
#include <stdint.h>
#include <utility>

typedef unsigned u32;
typedef unsigned long long u64;
typedef u32 u32x2 __attribute__((ext_vector_type(2)));
typedef u32 u32x4 __attribute__((ext_vector_type(4)));
typedef float f32x4 __attribute__((ext_vector_type(4)));
typedef __bf16 bf16x2 __attribute__((ext_vector_type(2)));
typedef __amdgpu_buffer_rsrc_t rsrc_t;
#define LDSP(T, off) ((__attribute__((address_space(3))) T*)(size_t)(off))
#define DEV __device__ __forceinline__

constexpr u32 WS_FLAGS = 0x1000, WS_FILL = 0x10000;
constexpr u32 OFF_P = 0;
constexpr u32 OFF_W = 524288;
constexpr u32 OFF_Q = OFF_W + 917504;
constexpr u32 OFF_H = OFF_Q + 32768;
constexpr u32 OFF_X1 = OFF_H + 16384;
constexpr u32 OFF_XIN = OFF_X1 + 4096;
constexpr u32 LAYER_SZ = OFF_XIN + 4096;
constexpr u32 FILL_PER_CU = 12288;
static_assert(2 * LAYER_SZ <= 256 * FILL_PER_CU, "fill covers both layers");
constexpr u32 SENT = 0xFFFFFFFFu;
#define OOB_IF(off, m) ((off) | ((m) << 24))
constexpr int SPIN_MAX = 30000;
constexpr u32 L_SRC = 0, L_STAT = 6144, L_TAG = 6400, L_STAMP = 6464, L_W = 8192, L_K = 65536, L_TOTAL = 163840;
constexpr int RB = 40, RS = 40, RR = 72, RK = 96;

constexpr int qk_t(int n) { return n < 16 ? (n & 3) : (4 + ((n - 16) >> 3) * 2 + (n & 1)); }
constexpr int qk_rho(int n) { return n < 16 ? (n >> 2) : (((n - 16) >> 1) & 3); }
constexpr int qk_J(int n) { return ((qk_t(n) & 3) - qk_rho(n)) & 3; }
constexpr bool qk_lds(int n) { return (n & 7) == 0 || (n & 7) == 3 || (n & 7) == 5; }
constexpr int qk_slot(int n) { int c = 0; for (int i = 0; i < n; i++) c += (qk_lds(i) == qk_lds(n)); return c; }
constexpr int qk_find(int t, int J) { for (int n = 0; n < 32; n++) if (qk_t(n) == t && qk_J(n) == J) return n; return -1; }

template <int N> DEV void wait_vm() { asm volatile("s_waitcnt vmcnt(%c0)" ::"i"(N > 63 ? 63 : N) : "memory"); }
template <int N> DEV void wait_lgkm() { asm volatile("s_waitcnt lgkmcnt(%c0)" ::"i"(N) : "memory"); }
template <int N> DEV void nop() { asm volatile("s_nop %c0" ::"i"(N)); }
DEV void lds_barrier() { asm volatile("s_waitcnt lgkmcnt(0)\n\ts_barrier" ::: "memory"); }

template <int T, int J8> DEV void ld_k1(u32 voff, rsrc_t rs, u32 soff, u32 kb) {
  constexpr int n = qk_find(T, J8 >> 1), sl = qk_slot(n);
  if constexpr (qk_lds(n)) {
    asm volatile("s_add_u32 m0, %3, %c5\n\ts_nop 0\n\tbuffer_load_dwordx4 %0, %1, %2 offen offset:%c4 nt lds" ::"v"(voff), "s"(rs), "s"(soff),
                 "s"(kb), "i"(J8 * 64), "i"(sl * 2048 + (J8 & 1) * 1024 - J8 * 64 + 512) : "memory", "scc");
  } else {
    constexpr int r = RK + sl * 8 + (J8 & 1) * 4;
    asm volatile("buffer_load_dwordx4 v[%c3:%c4], %0, %1, %2 offen offset:%c5 nt" ::"v"(voff), "s"(rs), "s"(soff), "i"(r), "i"(r + 3), "i"(J8 * 64) : "memory");
  }
}
template <int T, int... J> DEV void ld_k_(u32 voff, rsrc_t rs, u32 sbase, u32 kb, std::integer_sequence<int, J...>) {
  asm volatile("" : "+s"(sbase));
  u32 so = sbase + T * 32768;
  (ld_k1<T, J>(voff, rs, so, kb), ...);
}
template <int T> DEV void ld_k(u32 voff, rsrc_t rs, u32 sbase, u32 kb) { ld_k_<T>(voff, rs, sbase, kb, std::make_integer_sequence<int, 8>{}); }

template <int T, int A> DEV void ld_v4(u32 voff, rsrc_t rs, u32 soff) {
  constexpr int r = (T * 4 + A) * 8;
  asm volatile("buffer_load_dwordx2 a[%c3:%c4], %0, %1, %2 offen nt\n\t"
               "buffer_load_dwordx2 a[%c5:%c6], %0, %1, %2 offen offset:512 nt\n\t"
               "buffer_load_dwordx2 a[%c7:%c8], %0, %1, %2 offen offset:1024 nt\n\t"
               "buffer_load_dwordx2 a[%c9:%c10], %0, %1, %2 offen offset:1536 nt" ::"v"(voff), "s"(rs), "s"(soff),
               "i"(r), "i"(r + 1), "i"(r + 2), "i"(r + 3), "i"(r + 4), "i"(r + 5), "i"(r + 6), "i"(r + 7) : "memory");
}
template <int T> DEV void ld_v(u32 voff, rsrc_t rs, u32 sbase, int w) {
  asm volatile("" : "+s"(sbase), "+s"(w));
  u32 so = sbase + T * 32768;
  ld_v4<T, 0>(voff, rs, so + ((w + 0) & 3) * 128);
  ld_v4<T, 1>(voff, rs, so + ((w + 1) & 3) * 128);
  ld_v4<T, 2>(voff, rs, so + ((w + 2) & 3) * 128);
  ld_v4<T, 3>(voff, rs, so + ((w + 3) & 3) * 128);
}

template <int R, int OFF> DEV void ds_rd128(u32 addr) {
  asm volatile("ds_read_b128 v[%c1:%c2], %0 offset:%c3" ::"v"(addr), "i"(R), "i"(R + 3), "i"(OFF) : "memory");
}
template <int R, int OFF> DEV void ds_wr128(u32 addr) {
  asm volatile("ds_write_b128 %0, v[%c1:%c2] offset:%c3" ::"v"(addr), "i"(R), "i"(R + 3), "i"(OFF) : "memory");
}
template <int R> DEV void zero2() { asm volatile("v_mov_b64 v[%c0:%c1], 0" ::"i"(R), "i"(R + 1)); }
template <int R, int... I> DEV void zero_(std::integer_sequence<int, I...>) { (zero2<R + 2 * I>(), ...); }
template <int R, int N> DEV void zero_regs() { zero_<R>(std::make_integer_sequence<int, N / 2>{}); }

template <int F> DEV void w_rd1(u32 addr) {
  asm volatile("ds_read_b64_tr_b16 v[%c1:%c2], %0 offset:%c5\n\tds_read_b64_tr_b16 v[%c3:%c4], %0 offset:%c6" ::"v"(addr),
               "i"(RB + 4 * F), "i"(RB + 4 * F + 1), "i"(RB + 4 * F + 2), "i"(RB + 4 * F + 3), "i"(F * 1024), "i"(F * 1024 + 512) : "memory");
}
template <int... F> DEV void w_rd_(u32 addr, std::integer_sequence<int, F...>) { (w_rd1<F>(addr), ...); }
DEV void w_read(u32 addr) { w_rd_(addr, std::make_integer_sequence<int, 14>{}); wait_lgkm<0>(); }

DEV void w_dma(const char* src, u64 stride, u32 ldsb) {
#pragma unroll
  for (int f = 0; f < 14; f++) {
    asm volatile("s_add_u32 m0, %1, %c2\n\ts_nop 0\n\tglobal_load_lds_dwordx4 %0, off" ::"v"(src), "s"(ldsb), "i"(f * 1024) : "memory", "scc");
    src += stride;
  }
}

DEV float asf(u32 x) { return __builtin_bit_cast(float, x); }
DEV u32 asu(float x) { return __builtin_bit_cast(u32, x); }
DEV bf16x2 asb(u32 x) { return __builtin_bit_cast(bf16x2, x); }
template <int CTRL> DEV float dppf(float x) { return __builtin_bit_cast(float, __builtin_amdgcn_update_dpp(0, __builtin_bit_cast(int, x), CTRL, 0xf, 0xf, true)); }
constexpr int ROR(int n) { return 0x120 + n; }
constexpr int QP_X1 = 0xB1;
DEV void swap32(float& a, float& b) {
  auto r = __builtin_amdgcn_permlane32_swap(asu(a), asu(b), false, false);
  a = asf(r[0]); b = asf(r[1]);
}
DEV void swap16(float& a, float& b) {
  auto r = __builtin_amdgcn_permlane16_swap(asu(a), asu(b), false, false);
  a = asf(r[0]); b = asf(r[1]);
}
DEV u32 pk_bf16(float lo, float hi) { u32 r; asm("v_cvt_pk_bf16_f32 %0, %1, %2" : "=v"(r) : "v"(lo), "v"(hi)); return r; }
DEV u32 lo_bf16(float lo) { u32 r; asm("v_cvt_pk_bf16_f32 %0, %1, 0" : "=v"(r) : "v"(lo)); return r; }
template <u32 C> DEV u32 vconst() { u32 r; asm volatile("v_mov_b32 %0, %c1" : "=v"(r) : "i"(C)); return r; }
DEV u32 max4(u32x4 a) { return max(max(a.x, a.y), max(a.z, a.w)); }
DEV bool any_sent(u32 m) { return __builtin_amdgcn_ballot_w64(m == SENT) != 0; }
DEV u32x4 ld128(rsrc_t rs, u32 voff) { return __builtin_amdgcn_raw_buffer_load_b128(rs, voff, 0, 16); }
DEV u32 ld32(rsrc_t rs, u32 voff) { return __builtin_amdgcn_raw_buffer_load_b32(rs, voff, 0, 16); }
DEV void st32(rsrc_t rs, u32 voff, u32 v) { __builtin_amdgcn_raw_buffer_store_b32(v, rs, voff, 0, 16); }
DEV void st128(rsrc_t rs, u32 voff, u32x4 v) { __builtin_amdgcn_raw_buffer_store_b128(v, rs, voff, 0, 16); }
DEV rsrc_t mk_rs(const void* p, u32 n) { return __builtin_amdgcn_make_buffer_rsrc((void*)p, (short)0, (int)n, 0x00020000); }
#define USE4(a, b, c, d) asm volatile("" : "+v"(a), "+v"(b), "+v"(c), "+v"(d)::"memory")

template <int N> DEV void qk_step(u32x4 (&A)[4], float (&sc)[4], u32 idx, u32 kaddr) {
  constexpr int t = qk_t(N), rho = qk_rho(N), sl = qk_slot(N);
  constexpr int br = qk_lds(N) ? RR + (sl % 3) * 8 : RK + sl * 8;
  if constexpr (qk_lds(N) && sl >= 3) wait_lgkm<(sl <= 9 ? 4 : (11 - sl) * 2)>();
  asm volatile("v_smfmac_f32_16x16x64_bf16 v[%c2:%c3], %0, v[%c4:%c5], %1" ::"v"(A[rho]), "v"(idx), "i"(RS + 4 * t), "i"(RS + 4 * t + 3), "i"(br), "i"(br + 7));
  if constexpr (qk_lds(N) && sl + 3 < 12) {
    ds_rd128<br, (sl + 3) * 2048>(kaddr);
    ds_rd128<br + 4, (sl + 3) * 2048 + 1024>(kaddr);
  }
  if constexpr (N + 1 < 12) {
    constexpr int r = 1 + (N + 1) / 4, k = (N + 1) & 3;
    u32 d;
    asm volatile("v_mov_b32_dpp %0, %1 row_ror:%c2 row_mask:0xf bank_mask:0xf" : "=v"(d) : "v"(A[0][k]), "i"(4 * r));
    A[r][k] = d;
  }
  constexpr int c = (N >= 16 && N < 24) ? N - 16 : (N >= 26 && N < 30) ? N - 18 : -1;
  if constexpr (c >= 0) {
    constexpr int tt = c >> 1, h = c & 1;
    asm volatile("v_add_f32_dpp %0, v%c1, v%c2 quad_perm:[0,1,2,3] row_mask:%c3 bank_mask:0xf" : "+v"(sc[(tt >> 2) * 2 + h])
                 : "i"(RS + 4 * tt + 2 * h), "i"(RS + 4 * tt + 2 * h + 1), "i"(1 << (tt & 3)));
  }
}
template <int... N> DEV void qk_all(u32x4 (&A)[4], float (&sc)[4], u32 idx, u32 kaddr, std::integer_sequence<int, N...>) {
  (qk_step<N>(A, sc, idx, kaddr), ...);
}
template <int TT, int H> DEV void qk_compact(float (&sc)[4]) {
  asm volatile("v_add_f32_dpp %0, v%c1, v%c2 quad_perm:[0,1,2,3] row_mask:%c3 bank_mask:0xf" : "+v"(sc[(TT >> 2) * 2 + H])
               : "i"(RS + 4 * TT + 2 * H), "i"(RS + 4 * TT + 2 * H + 1), "i"(1 << (TT & 3)));
}
template <int T, int A> DEV void pv_step(u32 idx) {
  constexpr int b = (T * 4 + A) * 8;
  asm volatile("v_smfmac_f32_16x16x64_bf16 v[%c1:%c2], v[%c3:%c4], a[%c5:%c6], %0 abid:1" ::"v"(idx), "i"(RR + 4 * A), "i"(RR + 4 * A + 3),
               "i"(RS + 4 * T), "i"(RS + 4 * T + 3), "i"(b), "i"(b + 7));
}
template <int T> DEV void pv_tile(u32 idx) { pv_step<T, 0>(idx); pv_step<T, 1>(idx); pv_step<T, 2>(idx); pv_step<T, 3>(idx); }

DEV f32x4 vzero4() {
  u64 a, b;
  asm volatile("v_mov_b64 %0, 0" : "=v"(a));
  asm volatile("v_mov_b64 %0, 0" : "=v"(b));
  u32x2 lo = __builtin_bit_cast(u32x2, a), hi = __builtin_bit_cast(u32x2, b);
  u32x4 r = __builtin_shufflevector(lo, hi, 0, 1, 2, 3);
  return __builtin_bit_cast(f32x4, r);
}
DEV float gemv_core(const u32x4 (&x)[4], u32 idx, u32 lane, f32x4 a0, f32x4 a1) {
  asm volatile("s_nop 1\n\t"
               "v_smfmac_f32_16x16x64_bf16 %0, %2, v[40:47], %6\n\t"
               "v_smfmac_f32_16x16x64_bf16 %1, %2, v[48:55], %6\n\t"
               "v_smfmac_f32_16x16x64_bf16 %0, %3, v[56:63], %6\n\t"
               "v_smfmac_f32_16x16x64_bf16 %1, %3, v[64:71], %6\n\t"
               "v_smfmac_f32_16x16x64_bf16 %0, %4, v[72:79], %6\n\t"
               "v_smfmac_f32_16x16x64_bf16 %1, %4, v[80:87], %6\n\t"
               "v_smfmac_f32_16x16x64_bf16 %0, %5, v[88:95], %6\n\t"
               "s_nop 7"
               : "+v"(a0), "+v"(a1) : "v"(x[0]), "v"(x[1]), "v"(x[2]), "v"(x[3]), "v"(idx));
  const bool hi = lane & 32;
  float A = hi ? a1[0] + a1[1] : a0[0] + a0[1];
  float B = hi ? a1[2] + a1[3] : a0[2] + a0[3];
  swap32(A, B);
  return A + B;
}
DEV float colsum(u32 idx) {
  f32x4 a0 = vzero4(), a1 = vzero4();
  u32x4 one = {vconst<0x3F803F80u>(), vconst<0x3F803F80u>(), vconst<0x3F803F80u>(), vconst<0x3F803F80u>()};
  asm volatile("s_nop 1\n\t"
               "v_smfmac_f32_16x16x64_bf16 %0, %2, v[40:47], %3\n\t"
               "v_smfmac_f32_16x16x64_bf16 %1, %2, v[48:55], %3\n\t"
               "v_smfmac_f32_16x16x64_bf16 %0, %2, v[56:63], %3\n\t"
               "v_smfmac_f32_16x16x64_bf16 %1, %2, v[64:71], %3\n\t"
               "v_smfmac_f32_16x16x64_bf16 %0, %2, v[72:79], %3\n\t"
               "v_smfmac_f32_16x16x64_bf16 %1, %2, v[80:87], %3\n\t"
               "v_smfmac_f32_16x16x64_bf16 %0, %2, v[88:95], %3\n\t"
               "s_nop 7"
               : "+v"(a0), "+v"(a1) : "v"(one), "v"(idx));
  return (a0[0] + a0[1]) + (a1[0] + a1[1]);
}
DEV float gemv_ln(const u32x4 (&x)[4], u32 idx, float cs, u32 lane, f32x4 a0, f32x4 a1, float s1, float s2, float onef) {
  const bf16x2 one = __builtin_bit_cast(bf16x2, 0x3F803F80u);
#pragma unroll
  for (int u = 0; u < 4; u++)
#pragma unroll
    for (int k = 0; k < 4; k++) {
      bf16x2 v = asb(x[u][k]);
      s1 = __builtin_amdgcn_fdot2_f32_bf16(v, one, s1, false);
      s2 = __builtin_amdgcn_fdot2_f32_bf16(v, v, s2, false);
    }
  s1 += dppf<QP_X1>(s1);
  s2 += dppf<QP_X1>(s2);
  float z = (lane & 1) ? s2 : s1;
  z += dppf<ROR(8)>(z);
  f32x4 D;
  asm volatile("s_nop 1\n\tv_mfma_f32_16x16x4_f32 %0, %1, %2, 0" : "=&v"(D) : "v"(z), "v"(onef));
  float y = gemv_core(x, idx, lane, a0, a1);
  asm volatile("" : "+v"(D));
  const bool hi = lane & 32;
  float S1 = hi ? D[2] : D[0], S2 = hi ? D[3] : D[1];
  float m = S1 * (1.0f / 448.0f);
  float var = fmaxf(S2 * (1.0f / 448.0f) - m * m, 0.0f);
  float r = __builtin_amdgcn_rsqf(var + 1e-5f);
  return r * (y - m * cs);
}
#define UNDEF4(x) asm volatile("" : "=v"(x[0]), "=v"(x[1]), "=v"(x[2]), "=v"(x[3]))
template <int O1, int O2, int O3> DEV void ld4(u32x4 (&x)[4], rsrc_t rs, u32 v0, u32 v3, int n) {
  asm volatile("s_cmp_eq_u32 %7, 0\n\ts_cbranch_scc1 1f\n\t"
               "buffer_load_dwordx4 %0, %4, %6, 0 offen sc1\n\t"
               "buffer_load_dwordx4 %1, %4, %6, 0 offen offset:%c8 sc1\n\t"
               "buffer_load_dwordx4 %2, %4, %6, 0 offen offset:%c9 sc1\n\t"
               "buffer_load_dwordx4 %3, %5, %6, 0 offen offset:%c10 sc1\n\t"
               "s_waitcnt vmcnt(0)\n1:"
               : "+&v"(x[0]), "+&v"(x[1]), "+&v"(x[2]), "+&v"(x[3])
               : "v"(v0), "v"(v3), "s"(rs), "s"(n), "i"(O1), "i"(O2), "i"(O3) : "memory", "scc");
}
DEV void ld_x(u32x4 (&x)[4], rsrc_t rs, u32 v0, u32 v3, int n) { ld4<1024, 2048, 3072>(x, rs, v0, v3, n); }
DEV bool any_nan(float a, float b) { return __builtin_amdgcn_ballot_w64(__builtin_isunordered(a, b)) != 0; }
#define RETRY_IF(c) { if (__builtin_expect(!(c), 1)) break; asm volatile("s_nop 0"); if (--n <= 0) break; }
#define XLANE (g * 256 + (lane & 8) * 16 + (lane & 1) * 64 + ((lane >> 1) & 3) * 16)
#define BQ (2 * (g & 1) + (g >> 1))
DEV u32 xline(u32 nt) { return ((nt >> 3) * 8 + (nt & 3) * 2 + ((nt >> 2) & 1)) * 128; }

template <int T> DEV void wo_mfma(u32x4 B) {
  asm volatile("v_mfma_f32_16x16x32_bf16 v[%c1:%c2], v[%c1:%c2], %0, 0" ::"v"(B), "i"(RB + 4 * T), "i"(RB + 4 * T + 3));
}
template <int T> DEV void wo_store(u32 voff, rsrc_t rs) {
  asm volatile("s_mov_b32 m0, %c2\n\tbuffer_store_dwordx4 v[%c3:%c4], %0, %1, m0 offen sc1" ::"v"(voff), "s"(rs), "i"(T * 8192), "i"(RB + 4 * T), "i"(RB + 4 * T + 3) : "memory");
}
template <int T> DEV void wo_step(u32x4 B, u32 voff, rsrc_t rs) {
  if constexpr (T < 14) wo_mfma<T>(B); else nop<3>();
  if constexpr (T >= 3) wo_store<T - 3>(voff, rs);
}
template <int... T> DEV void wo_all(u32x4 B, u32 voff, rsrc_t rs, std::integer_sequence<int, T...>) { (wo_step<T>(B, voff, rs), ...); }

#define STAMP(i)

enum { R_NONE = 0, R_WO = 1, R_WQ = 2, R_GU = 3, R_DN = 4 };
#define LANE_VARS u32 lane; asm volatile("v_mbcnt_lo_u32_b32 %0, -1, 0\n\tv_mbcnt_hi_u32_b32 %0, -1, %0" : "=v"(lane)); const u32 c16 = lane & 15, g = lane >> 4; (void)c16; (void)g;
DEV int opq(int x) { asm volatile("" : "+s"(x)); return x; }

__global__ __launch_bounds__(256, 1) __attribute__((amdgpu_num_vgpr(40))) void fused_kernel(
    const char* __restrict__ xin, const char* __restrict__ kc, const char* __restrict__ vc, const char* __restrict__ wq,
    const char* __restrict__ wo, const char* __restrict__ wg, const char* __restrict__ wu, const char* __restrict__ wd,
    char* __restrict__ out, char* __restrict__ ws) {
  asm volatile("" ::: "v255", "a255");
  const u32 lane_ = threadIdx.x & 63;
  const int w = __builtin_amdgcn_readfirstlane(threadIdx.x >> 6);
  const int xcd = blockIdx.x & 7, ix = blockIdx.x >> 3;
  const int cu = xcd * 32 + ix;
  int pass = 0;
  STAMP(0);

  int role_ = R_NONE, rid_ = 0;
  if (ix < 28) {
    if (w == 0) { role_ = R_WO; rid_ = cu; }
    else if (w == 1) { role_ = R_WQ; rid_ = cu; }
    else if (w == 2) { role_ = R_GU; rid_ = xcd * 28 + ix; }
    else if (ix < 4) { role_ = R_WO; rid_ = xcd * 32 + 28 + ix; }
    else if (ix < 8) { role_ = R_WQ; rid_ = xcd * 32 + 24 + ix; }
  } else if (xcd < 7) { role_ = R_DN; rid_ = xcd * 4 + ix - 28; }
  const int red_ = xcd * 28 + ix;

  const rsrc_t rsK = mk_rs(kc, 0x4000000), rsV = mk_rs(vc, 0x4000000), rsX = mk_rs(xin, 3584);
  const u32 sbase = cu * 262144 + w * 8192;
  const u32 kbm = L_K + w * 24576 - 512, wb = L_W + w * 14336, sb = L_SRC + w * 1536;

  const char *wp0, *wp1;
  u32 wrow, wpiece;
  if (role_ == R_WO) { wp0 = wo + (rid_ >> 1) * (32 * 896) + (rid_ & 1) * 448; wp1 = wp0 + 16; wrow = 896; wpiece = 32; }
  else if (role_ == R_WQ) { wp0 = wq + rid_ * 32; wp1 = wp0 + 16; wrow = 8192; wpiece = 32 * 8192; }
  else if (role_ == R_GU) { wp0 = wg + rid_ * 16; wp1 = wu + rid_ * 16; wrow = 3584; wpiece = 32 * 3584; }
  else { wp0 = wd + w * (448 * 896) + rid_ * 32; wp1 = wp0 + 16; wrow = 896; wpiece = 32 * 896; }
  auto w_fetch = [&]() {
    LANE_VARS
    const u32 rho = lane >> 1, hf = rho >> 4, gb = (rho >> 2) & 3, q = rho & 3;
    const u32 rowg = 16 * (gb >> 1) + 8 * (q >> 1) + 4 * (gb & 1) + 2 * hf + (q & 1);
    const u32 rown = 8 * gb + 4 * hf + q;
    const u32 row = opq(wpiece) == 32 ? rown : rowg;
    w_dma(((lane & 1) ? wp1 : wp0) + (u64)row * opq(wrow), (u32)opq(wpiece), wb);
  };

  if (w == 3) {
    LANE_VARS
    const rsrc_t rsH = mk_rs(ws, WS_FILL + 256 * FILL_PER_CU);
    u32x2 e = __builtin_amdgcn_raw_buffer_load_b64(rsH, 0, 0, 16);
    const u32x4 s4 = {SENT, SENT, SENT, SENT};
#pragma unroll
    for (int i = 0; i < 12; i++) st128(rsH, WS_FILL + cu * FILL_PER_CU + i * 1024 + lane * 16, s4);
    u64 t = (((u64)e.y << 32) | e.x) * 6364136223846793005ull + 1442695040888963407ull;
    u32x2 tv = {(u32)t, (u32)(t >> 32)};
    wait_vm<0>();
    if (lane == 0) {
      __builtin_amdgcn_raw_buffer_store_b64(tv, rsH, WS_FLAGS + cu * 8, 0, 16);
      *LDSP(u32x2, L_TAG) = tv;
    }
  }
  if (role_ == R_WQ) w_fetch();
  {
    LANE_VARS
    const u32 voffK = c16 * 512 + g * 16;
    ld_k<0>(voffK, rsK, sbase, kbm); ld_k<1>(voffK, rsK, sbase, kbm); ld_k<2>(voffK, rsK, sbase, kbm);
  }

  const u32 idx = ((lane_ & 1) ? 0xEEEEu : 0x4444u) | (((lane_ & 3) * 0x5555u) << 16);

#pragma nounroll
  for (pass = 0; pass < 3; pass++) {
    pass = opq(pass);
    const int role = opq(role_);
    const bool dry = pass == 0, first = pass == 1;
    const int spins = pass * SPIN_MAX;
    const rsrc_t rs = mk_rs(ws + WS_FILL + (pass == 2 ? LAYER_SZ : 0), dry ? 0 : LAYER_SZ);
    const bool is_red = role == R_WQ && w == 1;
    u32 xres;
    asm volatile("" : "=v"(xres));
    STAMP(1);
    {
      u32x4 x[4];
      UNDEF4(x);
      if (first) {
        LANE_VARS
        const rsrc_t rsH = mk_rs(ws, WS_FILL);
        const u32x2 tg = *LDSP(u32x2, vconst<L_TAG>());
        u32x4 f0, f1;
        const u32 xo0 = ((lane >> 1) & 3) * 896 + (lane & 8) * 16 + g * 32 + (lane & 1) * 16;
        const u32 xo3 = OOB_IF(xo0, lane & 8);
        const u32 fo = WS_FLAGS + lane * 16;
        const u32 voffK = c16 * 512 + g * 16;
        asm volatile("buffer_load_dwordx4 %0, %6, %7, 0 offen sc1\n\t"
                     "buffer_load_dwordx4 %1, %6, %7, 0 offen offset:1024 sc1\n\t"
                     "buffer_load_dwordx4 %2, %8, %10, 0 offen\n\t"
                     "buffer_load_dwordx4 %3, %8, %10, 0 offen offset:256\n\t"
                     "buffer_load_dwordx4 %4, %8, %10, 0 offen offset:512\n\t"
                     "buffer_load_dwordx4 %5, %9, %10, 0 offen offset:768"
                     : "=&v"(f0), "=&v"(f1), "+&v"(x[0]), "+&v"(x[1]), "+&v"(x[2]), "+&v"(x[3])
                     : "v"(fo), "s"(rsH), "v"(xo0), "v"(xo3), "s"(rsX) : "memory");
        ld_k<3>(voffK, rsK, sbase, kbm); ld_k<4>(voffK, rsK, sbase, kbm);
        asm volatile("s_waitcnt vmcnt(16)" : "+v"(f0), "+v"(f1), "+v"(x[0]), "+v"(x[1]), "+v"(x[2]), "+v"(x[3])::"memory");
        for (int spin = 0;; spin++) {
          u32 d = ((f0.x ^ tg.x) | (f0.y ^ tg.y)) | ((f0.z ^ tg.x) | (f0.w ^ tg.y)) | ((f1.x ^ tg.x) | (f1.y ^ tg.y)) | ((f1.z ^ tg.x) | (f1.w ^ tg.y));
          if (__builtin_amdgcn_ballot_w64(d != 0) == 0 || spin > SPIN_MAX) break;
          __builtin_amdgcn_s_sleep(8);
          asm volatile("buffer_load_dwordx4 %0, %2, %3, 0 offen sc1\n\t"
                       "buffer_load_dwordx4 %1, %2, %3, 0 offen offset:1024 sc1\n\t"
                       "s_waitcnt vmcnt(0)" : "+&v"(f0), "+&v"(f1) : "v"(fo), "s"(rsH) : "memory");
        }
        if (cu == 0 && w == 0 && lane == 0) __builtin_amdgcn_raw_buffer_store_b64(tg, rsH, 0, 0, 16);
      }
      if (role == R_WQ) {
        LANE_VARS
        const int rid = opq(rid_);
        w_read(wb + lane * 8);
        float cs = colsum(idx);
        const u32 xo = OFF_XIN + XLANE;
        u32 qo = OFF_Q + (BQ * 8 + (rid >> 5)) * 1024 + ((rid & 3) * 16 + ((rid >> 2) & 3) * 4 + ((rid >> 4) & 1) * 2 + ((lane >> 1) & 1)) * 16 + (c16 >> 2) * 4;
        qo = OOB_IF(qo, lane & 1);
        asm volatile("" : "+v"(cs), "+v"(qo));
        for (int n = (pass >> 1) * SPIN_MAX;;) {
          const f32x4 a0 = vzero4(), a1 = vzero4();
          const float s1 = asf(vconst<0>()), s2 = asf(vconst<0>()), onef = asf(vconst<0x3F800000u>());
          ld_x(x, rs, xo, OOB_IF(xo, lane & 8), n);
          STAMP(2);
          float y = gemv_ln(x, idx, cs, lane, a0, a1, s1, s2, onef) * (0.0625f * 1.4426950408889634f);
          st32(rs, qo, pk_bf16(y, dppf<QP_X1>(y)));
          RETRY_IF(any_nan(y, y));
        }
        STAMP(3);
      }
    }
    if (is_red && !dry) {
      LANE_VARS
      const int red = opq(red_);
      const u32 b = red / 56, q8 = red % 56, dw = (lane & 1) * 2 + (lane >> 5);
      asm volatile("buffer_load_dword %0, %1, %2, 0 offen sc1" : "+&v"(xres)
                   : "v"((first ? b * 896 + q8 * 16 : OFF_XIN + xline(q8 >> 1) + ((q8 & 1) * 4 + b) * 16) + dw * 4), "s"(first ? rsX : rs) : "memory");
    }
    {
      LANE_VARS
      u32x4 A[4];
      asm volatile("" : "=v"(A[0]));
      const u32 qo = OFF_Q + (cu >> 3) * 1024 + lane * 16;
      const u32 voffV = g * 2048 + c16 * 8;
      const u32 kaddr = kbm + 512 + lane * 16;
      zero_regs<RS, 32>();
      if (first) {
        const u32 voffK = c16 * 512 + g * 16;
        ld_k<5>(voffK, rsK, sbase, kbm); ld_k<6>(voffK, rsK, sbase, kbm); ld_k<7>(voffK, rsK, sbase, kbm);
        ld_v<0>(voffV, rsV, sbase, w);
        wait_vm<16>();
        asm volatile("buffer_load_dwordx4 %0, %1, %2, 0 offen sc1" : "+&v"(A[0]) : "v"(qo), "s"(rs) : "memory");
        ld_v<1>(voffV, rsV, sbase, w); ld_v<2>(voffV, rsV, sbase, w);
        asm volatile("s_waitcnt vmcnt(32)" : "+v"(A[0])::"memory");
      }
      ds_rd128<RR, 0>(kaddr); ds_rd128<RR + 4, 1024>(kaddr); ds_rd128<RR + 8, 2048>(kaddr);
      ds_rd128<RR + 12, 3072>(kaddr); ds_rd128<RR + 16, 4096>(kaddr); ds_rd128<RR + 20, 5120>(kaddr);
      const u32 kap = 8 * ((c16 >> 1) & 1) + 2 * (c16 >> 2) + (c16 & 1);
      u32 pa = sb + g * 128 + kap * 4, ra = sb + ((lane >> 2) & 1) * 64 + g * 16;
      float sc[4];
      asm volatile("" : "+v"(pa), "+v"(ra), "=v"(sc[0]), "=v"(sc[1]), "=v"(sc[2]), "=v"(sc[3]));
      wait_lgkm<0>();
      for (int n = spins, go = pass >> 1;; go = 1) {
        asm volatile("s_cmp_eq_u32 %3, 0\n\ts_cbranch_scc1 1f\n\t"
                     "buffer_load_dwordx4 %0, %1, %2, 0 offen sc1\n\ts_waitcnt vmcnt(0)\n1:"
                     : "+&v"(A[0]) : "v"(qo), "s"(rs), "s"(go) : "memory", "scc");
        RETRY_IF(__builtin_amdgcn_ballot_w64(__builtin_isunordered(asf(A[0].x), asf(A[0].y)) | __builtin_isunordered(asf(A[0].z), asf(A[0].w))) != 0);
      }
      asm volatile("" : "+v"(xres));
      STAMP(4);
      { u32 d; asm volatile("s_nop 1\n\tv_mov_b32_dpp %0, %1 row_ror:4 row_mask:0xf bank_mask:0xf" : "=v"(d) : "v"(A[0][0])); A[1][0] = d; }
      qk_all(A, sc, idx, kaddr, std::make_integer_sequence<int, 32>{});
      nop<7>();
      qk_compact<6, 0>(sc); qk_compact<6, 1>(sc); qk_compact<7, 0>(sc); qk_compact<7, 1>(sc);
      float m0 = fmaxf(sc[0], sc[2]), m1 = fmaxf(sc[1], sc[3]);
      swap32(m0, m1);
      float mx = fmaxf(m0, m1), my = mx;
      swap16(mx, my);
      mx = fmaxf(mx, my);
      mx = fmaxf(mx, dppf<ROR(8)>(mx)); mx = fmaxf(mx, dppf<ROR(4)>(mx)); mx = fmaxf(mx, dppf<ROR(2)>(mx)); mx = fmaxf(mx, dppf<ROR(1)>(mx));
      const float M0 = asf(__builtin_amdgcn_readlane(asu(mx), 0)), M1 = asf(__builtin_amdgcn_readlane(asu(mx), 32));
      const float p00 = __builtin_amdgcn_exp2f(sc[0] - M0), p01 = __builtin_amdgcn_exp2f(sc[1] - M1);
      const float p10 = __builtin_amdgcn_exp2f(sc[2] - M0), p11 = __builtin_amdgcn_exp2f(sc[3] - M1);
      asm volatile("ds_write2_b32 %0, %1, %2 offset0:0 offset1:16\n\tds_write2_b32 %0, %3, %4 offset0:128 offset1:144" ::"v"(pa),
                   "v"(lo_bf16(p00)), "v"(lo_bf16(p01)), "v"(lo_bf16(p10)), "v"(lo_bf16(p11)) : "memory");
      ds_rd128<RS, 0>(ra); ds_rd128<RS + 4, 128>(ra); ds_rd128<RS + 8, 256>(ra); ds_rd128<RS + 12, 384>(ra);
      ds_rd128<RS + 16, 512>(ra); ds_rd128<RS + 20, 640>(ra); ds_rd128<RS + 24, 768>(ra); ds_rd128<RS + 28, 896>(ra);
      zero_regs<RR, 16>();
      float l0 = p00 + p10, l1 = p01 + p11;
      swap32(l0, l1);
      float lx = l0 + l1, ly = lx;
      swap16(lx, ly);
      lx += ly;
      lx += dppf<ROR(8)>(lx); lx += dppf<ROR(4)>(lx); lx += dppf<ROR(2)>(lx); lx += dppf<ROR(1)>(lx);
      if ((lane & 28) == 0) {
        const u32 d = lane & 3, h = lane >> 5;
        *LDSP(u32x2, L_STAT + ((d * 2 + h) * 4 + ((d - w) & 3)) * 8) = u32x2{asu(mx), asu(lx)};
      }
      const u32 l32 = (lane & 31) * 16;
      u32 oa = sb + l32, o1 = ((w + 3) & 3) * 1536 + l32, o2 = ((w + 2) & 3) * 1536 + 512 + l32, o3 = ((w + 1) & 3) * 1536 + 1024 + l32;
      u32 sa = L_STAT + (w * 2 + (g & 1)) * 32;
      u32 po = OFF_P + ((((cu >> 3) & 7) * 2 + (g & 1)) * 8 + 2 * w + (c16 >> 3)) * 4096 + ((cu & 7) * 4 + (cu >> 6)) * 128 + (c16 & 1) * 64 + ((c16 >> 1) & 3) * 16;
      po = OOB_IF(po, lane & 32);
      asm volatile("" : "+v"(oa), "+v"(o1), "+v"(o2), "+v"(o3), "+v"(sa), "+v"(po));
      STAMP(5);
      wait_lgkm<0>();
      if (first) {
        wait_vm<32>(); ld_v<3>(voffV, rsV, sbase, w); pv_tile<0>(idx);
        wait_vm<32>(); ld_v<4>(voffV, rsV, sbase, w); pv_tile<1>(idx);
        wait_vm<32>(); ld_v<5>(voffV, rsV, sbase, w); pv_tile<2>(idx);
        wait_vm<32>(); ld_v<6>(voffV, rsV, sbase, w); pv_tile<3>(idx);
        wait_vm<32>(); ld_v<7>(voffV, rsV, sbase, w); pv_tile<4>(idx);
        wait_vm<32>();
        if (role == R_WO || role == R_GU || role == R_DN) w_fetch();
        else {
#pragma unroll
          for (int i = 0; i < 14; i++) asm volatile("buffer_load_dword v%c1, off, %0, 0" ::"s"(rsX), "i"(RR + 23) : "memory");
        }
        pv_tile<5>(idx);
      } else {
        pv_tile<0>(idx); pv_tile<1>(idx); pv_tile<2>(idx); pv_tile<3>(idx); pv_tile<4>(idx); pv_tile<5>(idx);
      }
      wait_vm<30>(); pv_tile<6>(idx);
      wait_vm<26>(); pv_step<7, 0>(idx);
      wait_vm<22>(); pv_step<7, 1>(idx);
      wait_vm<18>(); pv_step<7, 2>(idx);
      wait_vm<14>(); pv_step<7, 3>(idx);
      nop<7>();
      ds_wr128<RR + 4, 0>(oa); ds_wr128<RR + 8, 512>(oa); ds_wr128<RR + 12, 1024>(oa);
      STAMP(6);
      lds_barrier();
      const f32x4 s01 = *LDSP(f32x4, sa), s23 = *LDSP(f32x4, sa + 16);
      const f32x4 v1 = *LDSP(f32x4, o1), v2 = *LDSP(f32x4, o2), v3 = *LDSP(f32x4, o3);
      const float M = fmaxf(fmaxf(s01[0], s01[2]), fmaxf(s23[0], s23[2]));
      float f0 = __builtin_amdgcn_exp2f(s01[0] - M), f1 = __builtin_amdgcn_exp2f(s01[2] - M);
      float f2 = __builtin_amdgcn_exp2f(s23[0] - M), f3 = __builtin_amdgcn_exp2f(s23[2] - M);
      const float L = (f0 * s01[1] + f1 * s01[3]) + (f2 * s23[1] + f3 * s23[3]);
      const float rl = __builtin_amdgcn_rcpf(L);
      const u32 lse = asu(M + __builtin_amdgcn_logf(L));
      f0 *= rl; f1 *= rl; f2 *= rl; f3 *= rl;
      float o[4];
      asm volatile("v_mul_f32 %0, %4, v72\n\tv_mul_f32 %1, %4, v73\n\tv_mul_f32 %2, %4, v74\n\tv_mul_f32 %3, %4, v75"
                   : "=&v"(o[0]), "=&v"(o[1]), "=&v"(o[2]), "=&v"(o[3]) : "v"(f0));
      u32x4 pv;
#pragma unroll
      for (int i = 0; i < 4; i++) {
        float t = __builtin_fmaf(f3, v3[i], __builtin_fmaf(f2, v2[i], __builtin_fmaf(f1, v1[i], o[i])));
        pv[i] = __builtin_amdgcn_perm(asu(t), lse, 0x07060500u + i);
      }
      st128(rs, po, pv);
      STAMP(7);
      if (!dry) wait_vm<1>();
    }
    if (role == R_WO) {
      LANE_VARS
      const int rid = opq(rid_);
      w_read(wb + lane * 8);
      const u32 go = OFF_P + (rid >> 1) * 4096 + c16 * 128 + g * 16;
      u32 so = OFF_W + (((c16 & 3) * 56 + (rid & 1) * 28 + (g >> 1)) * 128 + (rid >> 1)) * 32 + (g & 1) * 16;
      so = OOB_IF(so, lane & 12);
      asm volatile("" : "+v"(so));
      u32x4 x[4];
      UNDEF4(x);
      float o[8], rd;
      for (int n = spins;;) {
        ld4<64, 2048, 2112>(x, rs, go, go, n);
        const u32x4 a0 = x[0], a1 = x[1], b0 = x[2], b1 = x[3];
        STAMP(8);
        const float la = asf(__builtin_amdgcn_perm(__builtin_amdgcn_perm(a0.w, a0.z, 0x0c0c0400u), __builtin_amdgcn_perm(a0.y, a0.x, 0x0c0c0400u), 0x05040100u));
        const float lb = asf(__builtin_amdgcn_perm(__builtin_amdgcn_perm(b0.w, b0.z, 0x0c0c0400u), __builtin_amdgcn_perm(b0.y, b0.x, 0x0c0c0400u), 0x05040100u));
        float M = fmaxf(la, lb);
        M = fmaxf(M, dppf<ROR(4)>(M)); M = fmaxf(M, dppf<ROR(8)>(M));
        const float wa = __builtin_amdgcn_exp2f(la - M), wb2 = __builtin_amdgcn_exp2f(lb - M);
        float den = wa + wb2;
        den += dppf<ROR(4)>(den); den += dppf<ROR(8)>(den);
        rd = __builtin_amdgcn_rcpf(den);
#pragma unroll
        for (int i = 0; i < 4; i++) {
          o[i] = __builtin_fmaf(wb2, asf(b0[i]), wa * asf(a0[i]));
          o[4 + i] = __builtin_fmaf(wb2, asf(b1[i]), wa * asf(a1[i]));
        }
        RETRY_IF(any_nan(o[4], o[7]));
      }
#pragma unroll
      for (int i = 0; i < 8; i++) { o[i] += dppf<ROR(4)>(o[i]); }
#pragma unroll
      for (int i = 0; i < 8; i++) { o[i] += dppf<ROR(8)>(o[i]); o[i] *= rd; }
      u32x4 B = {pk_bf16(o[0], o[1]), pk_bf16(o[2], o[3]), pk_bf16(o[4], o[5]), pk_bf16(o[6], o[7])};
      asm volatile("s_nop 1" : "+v"(B));
      wo_all(B, so, rs, std::make_integer_sequence<int, 17>{});
      nop<1>();
      STAMP(9);
    }
    if (is_red) {
      LANE_VARS
      const int red = opq(red_);
      const u32 b = red / 56, q8 = red % 56;
      const u32 go = OFF_W + red * 4096 + lane * 16;
      u32 so = OFF_X1 + xline(q8 >> 1) + ((q8 & 1) * 4 + b) * 16 + ((lane & 1) * 2 + (lane >> 5)) * 4;
      so = OOB_IF(so, lane & 30);
      asm volatile("" : "+v"(so));
      u32x4 x[4];
      UNDEF4(x);
      for (int n = spins;;) {
        ld_x(x, rs, go, go, n);
        STAMP(10);
        float s[4];
#pragma unroll
        for (int i = 0; i < 4; i++) s[i] = (asf(x[0][i]) + asf(x[1][i])) + (asf(x[2][i]) + asf(x[3][i]));
        swap32(s[0], s[2]); swap32(s[1], s[3]);
        float a = s[0] + s[2], c = s[1] + s[3], a2 = a, c2 = c;
        swap16(a, a2); swap16(c, c2);
        a += a2; c += c2;
        a += dppf<ROR(8)>(a); c += dppf<ROR(8)>(c);
        a += dppf<ROR(4)>(a); c += dppf<ROR(4)>(c);
        a += dppf<ROR(2)>(a); c += dppf<ROR(2)>(c);
        st32(rs, so, pk_bf16(a + asf(xres << 16), c + asf(xres & 0xFFFF0000u)));
        RETRY_IF(any_nan(a, c));
      }
      STAMP(11);
    }
    if (role == R_GU) {
      LANE_VARS
      const int rid = opq(rid_);
      w_read(wb + lane * 8);
      float cs = colsum(idx);
      const u32 xo = OFF_X1 + XLANE;
      const u32 q8 = rid % 56;
      u32 so = OFF_H + (rid / 56) * 4096 + xline(q8 >> 1) + ((q8 & 1) * 4 + BQ) * 16 + (c16 >> 1) * 4;
      so = OOB_IF(so, lane & 9);
      asm volatile("" : "+v"(cs), "+v"(so));
      u32x4 x[4];
      UNDEF4(x);
      for (int n = spins;;) {
        const f32x4 a0 = vzero4(), a1 = vzero4();
        const float s1 = asf(vconst<0>()), s2 = asf(vconst<0>()), onef = asf(vconst<0x3F800000u>());
        ld_x(x, rs, xo, OOB_IF(xo, lane & 8), n);
        STAMP(12);
        const float y = gemv_ln(x, idx, cs, lane, a0, a1, s1, s2, onef);
        const float e = __builtin_amdgcn_exp2f(y * __builtin_fmaf(y * y, -0.044715f * 2.3022081986f, -2.3022081986f));
        const float h = y * __builtin_amdgcn_rcpf(1.0f + e) * dppf<ROR(8)>(y);
        st32(rs, so, pk_bf16(h, dppf<QP_X1>(h)));
        RETRY_IF(any_nan(y, y));
      }
      STAMP(13);
    }
    if (role == R_DN) {
      LANE_VARS
      const int rid = opq(rid_);
      w_read(wb + lane * 8);
      const u32 xo = OFF_H + w * 4096 + XLANE;
      const u32 ro = xline(rid) + ((c16 >> 3) * 4 + BQ) * 16 + ((c16 & 7) >> 1) * 4;
      u32 so = (pass == 2) ? BQ * 896 + rid * 32 + c16 * 2 : OFF_XIN + ro;
      so = OOB_IF(so, lane & 1);
      u32 la = lane * 4;
      asm volatile("" : "+v"(so), "+v"(la));
      const rsrc_t rso = mk_rs(pass == 2 ? out : ws + WS_FILL + LAYER_SZ, dry ? 0 : (pass == 2 ? 3584 : LAYER_SZ));
      u32 xr;
      asm volatile("" : "=v"(xr));
      for (int n = spins;;) {
        asm volatile("s_cmp_eq_u32 %3, 0\n\ts_cbranch_scc1 1f\n\t"
                     "buffer_load_dword %0, %1, %2, 0 offen sc1\n\ts_waitcnt vmcnt(0)\n1:"
                     : "+&v"(xr) : "v"(OFF_X1 + ro), "s"(rs), "s"(n) : "memory", "scc");
        RETRY_IF(any_nan(asf(xr), asf(xr << 16)));
      }
      u32x4 x[4];
      UNDEF4(x);
      float y;
      for (int n = spins;;) {
        const f32x4 a0 = vzero4(), a1 = vzero4();
        ld_x(x, rs, xo, OOB_IF(xo, lane & 8), n);
        STAMP(12);
        y = gemv_core(x, idx, lane, a0, a1);
        *LDSP(float, sb + la) = y;
        RETRY_IF(any_nan(y, y));
      }
      lds_barrier();
      if (w == 0) {
        y = (y + *LDSP(float, 1536 + la)) + (*LDSP(float, 3072 + la) + *LDSP(float, 4608 + la));
        st32(rso, so, pk_bf16(y + asf(xr << 16), dppf<QP_X1>(y) + asf(xr & 0xFFFF0000u)));
      }
      STAMP(13);
    }
  }
}

extern "C" void kernel_launch(void* const* d_in, const int* in_sizes, int n_in, void* d_out, int out_size, void* d_ws,
                              size_t ws_size, hipStream_t stream) {
  (void)hipFuncSetAttribute((const void*)fused_kernel, hipFuncAttributeMaxDynamicSharedMemorySize, L_TOTAL);
  fused_kernel<<<256, 256, L_TOTAL, stream>>>((const char*)d_in[0], (const char*)d_in[1], (const char*)d_in[2], (const char*)d_in[3],
                                              (const char*)d_in[4], (const char*)d_in[6], (const char*)d_in[7], (const char*)d_in[8],
                                              (char*)d_out, (char*)d_ws);
}
```

```cpp
#include <hip/hip_runtime.h>
#include <stdint.h>
#include <utility>

typedef unsigned u32;
typedef unsigned long long u64;
typedef u32 u32x2 __attribute__((ext_vector_type(2)));
typedef u32 u32x4 __attribute__((ext_vector_type(4)));
typedef float f32x4 __attribute__((ext_vector_type(4)));
typedef __bf16 bf16x2 __attribute__((ext_vector_type(2)));
typedef __amdgpu_buffer_rsrc_t rsrc_t;
#define LDSP(T, off) ((__attribute__((address_space(3))) T*)(size_t)(off))
#define DEV __device__ __forceinline__
#define NOUNROLL _Pragma("nounroll")

constexpr u32 WS_FLAGS = 0x1000, WS_FILL = 0x10000;
constexpr u32 OFF_P = 0;
constexpr u32 OFF_W = 524288;
constexpr u32 OFF_Q = OFF_W + 458752;
constexpr u32 OFF_H = OFF_Q + 32768;
constexpr u32 OFF_X1 = OFF_H + 16384;
constexpr u32 OFF_XIN = OFF_X1 + 32768;
constexpr u32 LAYER_SZ = OFF_XIN + 32768;
constexpr u32 FILL_PER_CU = 9216;
static_assert(2 * LAYER_SZ <= 256 * FILL_PER_CU, "fill covers both layers");
constexpr u32 SENT = 0xFFFFFFFFu;
#define OOB_IF(off, m) ((off) | ((m) << 24))
constexpr int SPIN_MAX = 30000;
#define D_WO 0
#define D_RED 40
#define D_GU 36
#define D_H 36
#define D_XIN 36
#define D_Q 4
#define D_Q2 36
#define WPOL " nt"
#define DN_LATE 0
#define WQ_FIRST 0
#define FILLPOL 16
#define WQMAP 2
#define GU_LATE 0
constexpr u32 L_SRC = 0, L_STAT = 6144, L_EV = 6400, L_STAMP = 6464, L_W = 8192, L_K = 65536, L_TOTAL = 163840;
constexpr int RT = 38, RB = 40, RS = 40, RR = 72, RK = 96;

constexpr int qk_t(int n) { return n < 16 ? (n & 3) : (4 + ((n - 16) >> 3) * 2 + (n & 1)); }
constexpr int qk_rho(int n) { return n < 16 ? (n >> 2) : (((n - 16) >> 1) & 3); }
constexpr int qk_J(int n) { return ((qk_t(n) & 3) - qk_rho(n)) & 3; }
constexpr bool qk_lds(int n) { return (n & 7) == 0 || (n & 7) == 3 || (n & 7) == 5; }
constexpr int qk_slot(int n) { int c = 0; for (int i = 0; i < n; i++) c += (qk_lds(i) == qk_lds(n)); return c; }
constexpr int qk_find(int t, int J) { for (int n = 0; n < 32; n++) if (qk_t(n) == t && qk_J(n) == J) return n; return -1; }

template <int N> DEV void wait_vm() { asm volatile("s_waitcnt vmcnt(%c0)" ::"i"(N > 63 ? 63 : N) : "memory"); }
template <int N> DEV void wait_lgkm() { asm volatile("s_waitcnt lgkmcnt(%c0)" ::"i"(N) : "memory"); }
template <int N> DEV void nop() { asm volatile("s_nop %c0" ::"i"(N)); }
DEV void lds_barrier() { asm volatile("s_waitcnt lgkmcnt(0)\n\ts_barrier" ::: "memory"); }

template <int T, int J8> DEV void ld_k1(u32 voff, rsrc_t rs, u32 soff, u32 kb) {
  constexpr int n = qk_find(T, J8 >> 1), sl = qk_slot(n);
  if constexpr (qk_lds(n)) {
    asm volatile("s_add_u32 m0, %3, %c5\n\ts_nop 0\n\tbuffer_load_dwordx4 %0, %1, %2 offen offset:%c4 nt lds" ::"v"(voff), "s"(rs), "s"(soff),
                 "s"(kb), "i"(J8 * 64), "i"(sl * 2048 + (J8 & 1) * 1024 - J8 * 64 + 512) : "memory", "scc");
  } else {
    constexpr int r = RK + sl * 8 + (J8 & 1) * 4;
    asm volatile("buffer_load_dwordx4 v[%c3:%c4], %0, %1, %2 offen offset:%c5 nt" ::"v"(voff), "s"(rs), "s"(soff), "i"(r), "i"(r + 3), "i"(J8 * 64) : "memory");
  }
}
template <int T, int... J> DEV void ld_k_(u32 voff, rsrc_t rs, u32 sbase, u32 kb, std::integer_sequence<int, J...>) {
  asm volatile("" : "+s"(sbase));
  u32 so = sbase + T * 32768;
  (ld_k1<T, J>(voff, rs, so, kb), ...);
}
template <int T> DEV void ld_k(u32 voff, rsrc_t rs, u32 sbase, u32 kb) { ld_k_<T>(voff, rs, sbase, kb, std::make_integer_sequence<int, 8>{}); }

template <int T, int A> DEV void ld_v4(u32 voff, rsrc_t rs, u32 soff) {
  constexpr int r = (T * 4 + A) * 8;
  asm volatile("buffer_load_dwordx2 a[%c3:%c4], %0, %1, %2 offen nt\n\t"
               "buffer_load_dwordx2 a[%c5:%c6], %0, %1, %2 offen offset:512 nt\n\t"
               "buffer_load_dwordx2 a[%c7:%c8], %0, %1, %2 offen offset:1024 nt\n\t"
               "buffer_load_dwordx2 a[%c9:%c10], %0, %1, %2 offen offset:1536 nt" ::"v"(voff), "s"(rs), "s"(soff),
               "i"(r), "i"(r + 1), "i"(r + 2), "i"(r + 3), "i"(r + 4), "i"(r + 5), "i"(r + 6), "i"(r + 7) : "memory");
}
template <int T> DEV void ld_v(u32 voff, rsrc_t rs, u32 sbase, int w) {
  asm volatile("" : "+s"(sbase), "+s"(w));
  u32 so = sbase + T * 32768;
  ld_v4<T, 0>(voff, rs, so + ((w + 0) & 3) * 128);
  ld_v4<T, 1>(voff, rs, so + ((w + 1) & 3) * 128);
  ld_v4<T, 2>(voff, rs, so + ((w + 2) & 3) * 128);
  ld_v4<T, 3>(voff, rs, so + ((w + 3) & 3) * 128);
}

template <int R, int OFF> DEV void ds_rd128(u32 addr) {
  asm volatile("ds_read_b128 v[%c1:%c2], %0 offset:%c3" ::"v"(addr), "i"(R), "i"(R + 3), "i"(OFF) : "memory");
}
template <int R, int OFF> DEV void ds_wr128(u32 addr) {
  asm volatile("ds_write_b128 %0, v[%c1:%c2] offset:%c3" ::"v"(addr), "i"(R), "i"(R + 3), "i"(OFF) : "memory");
}
template <int R> DEV void zero2() { asm volatile("v_mov_b64 v[%c0:%c1], 0" ::"i"(R), "i"(R + 1)); }
template <int R, int... I> DEV void zero_(std::integer_sequence<int, I...>) { (zero2<R + 2 * I>(), ...); }
template <int R, int N> DEV void zero_regs() { zero_<R>(std::make_integer_sequence<int, N / 2>{}); }

template <int F, int SF, int SH> DEV void w_rd1(u32 addr) {
  asm volatile("ds_read_b64_tr_b16 v[%c1:%c2], %0 offset:%c5\n\tds_read_b64_tr_b16 v[%c3:%c4], %0 offset:%c6" ::"v"(addr),
               "i"(RB + 4 * F), "i"(RB + 4 * F + 1), "i"(RB + 4 * F + 2), "i"(RB + 4 * F + 3), "i"(F * SF), "i"(F * SF + SH) : "memory");
}
template <int SF, int SH, int... F> DEV void w_rd_(u32 addr, std::integer_sequence<int, F...>) { (w_rd1<F, SF, SH>(addr), ...); }
template <int SF = 1024, int SH = 512> DEV void w_read(u32 addr) { w_rd_<SF, SH>(addr, std::make_integer_sequence<int, 14>{}); wait_lgkm<0>(); }
DEV void dma16(const char* src, u32 lds) {
  asm volatile("s_mov_b32 m0, %1\n\ts_nop 0\n\tglobal_load_lds_dwordx4 %0, off" WPOL ::"v"(src), "s"(lds) : "memory");
}

DEV float asf(u32 x) { return __builtin_bit_cast(float, x); }
DEV u32 asu(float x) { return __builtin_bit_cast(u32, x); }
DEV bf16x2 asb(u32 x) { return __builtin_bit_cast(bf16x2, x); }
template <int CTRL> DEV float dppf(float x) { return __builtin_bit_cast(float, __builtin_amdgcn_update_dpp(0, __builtin_bit_cast(int, x), CTRL, 0xf, 0xf, true)); }
constexpr int ROR(int n) { return 0x120 + n; }
constexpr int QP_X1 = 0xB1;
DEV void swap32(float& a, float& b) {
  auto r = __builtin_amdgcn_permlane32_swap(asu(a), asu(b), false, false);
  a = asf(r[0]); b = asf(r[1]);
}
DEV void swap16(float& a, float& b) {
  auto r = __builtin_amdgcn_permlane16_swap(asu(a), asu(b), false, false);
  a = asf(r[0]); b = asf(r[1]);
}
DEV u32 pk_bf16(float lo, float hi) { u32 r; asm("v_cvt_pk_bf16_f32 %0, %1, %2" : "=v"(r) : "v"(lo), "v"(hi)); return r; }
DEV u32 lo_bf16(float lo) { u32 r; asm("v_cvt_pk_bf16_f32 %0, %1, 0" : "=v"(r) : "v"(lo)); return r; }
template <u32 C> DEV u32 vconst() { u32 r; asm volatile("v_mov_b32 %0, %c1" : "=v"(r) : "i"(C)); return r; }
DEV u64 nan_mask(float a, float b) { return __builtin_amdgcn_ballot_w64(__builtin_isunordered(a, b)); }
DEV u32 max4(u32x4 a) { return max(max(a.x, a.y), max(a.z, a.w)); }
DEV bool any_sent(u32 m) { return __builtin_amdgcn_ballot_w64(m == SENT) != 0; }
DEV u32x4 ld128(rsrc_t rs, u32 voff) { return __builtin_amdgcn_raw_buffer_load_b128(rs, voff, 0, 16); }
DEV u32 ld32(rsrc_t rs, u32 voff) { return __builtin_amdgcn_raw_buffer_load_b32(rs, voff, 0, 16); }
DEV void st32(rsrc_t rs, u32 voff, u32 v) { __builtin_amdgcn_raw_buffer_store_b32(v, rs, voff, 0, 16); }
DEV void st16(rsrc_t rs, u32 voff, u32 soff, float v) {
  asm volatile("buffer_store_short %0, %1, %2, %3 offen sc1" ::"v"(lo_bf16(v)), "v"(voff), "s"(rs), "s"(soff) : "memory");
}
DEV void st128(rsrc_t rs, u32 voff, u32x4 v) { __builtin_amdgcn_raw_buffer_store_b128(v, rs, voff, 0, 16); }
DEV rsrc_t mk_rs(const void* p, u32 n) { return __builtin_amdgcn_make_buffer_rsrc((void*)p, (short)0, (int)n, 0x00020000); }
#define USE4(a, b, c, d) asm volatile("" : "+v"(a), "+v"(b), "+v"(c), "+v"(d)::"memory")

template <int N> DEV void qk_step(u32x4 (&A)[4], float (&sc)[4], u32 idx, u32 kaddr) {
  constexpr int t = qk_t(N), rho = qk_rho(N), sl = qk_slot(N);
  constexpr int br = qk_lds(N) ? RR + (sl % 3) * 8 : RK + sl * 8;
  if constexpr (qk_lds(N) && sl >= 3) wait_lgkm<(sl <= 9 ? 4 : (11 - sl) * 2)>();
  asm volatile("v_smfmac_f32_16x16x64_bf16 v[%c2:%c3], %0, v[%c4:%c5], %1" ::"v"(A[rho]), "v"(idx), "i"(RS + 4 * t), "i"(RS + 4 * t + 3), "i"(br), "i"(br + 7));
  if constexpr (qk_lds(N) && sl + 3 < 12) {
    ds_rd128<br, (sl + 3) * 2048>(kaddr);
    ds_rd128<br + 4, (sl + 3) * 2048 + 1024>(kaddr);
  }
  if constexpr (N + 1 < 12) {
    constexpr int r = 1 + (N + 1) / 4, k = (N + 1) & 3;
    u32 d;
    asm volatile("v_mov_b32_dpp %0, %1 row_ror:%c2 row_mask:0xf bank_mask:0xf" : "=v"(d) : "v"(A[0][k]), "i"(4 * r));
    A[r][k] = d;
  }
  constexpr int c = (N >= 16 && N < 24) ? N - 16 : (N >= 26 && N < 30) ? N - 18 : -1;
  if constexpr (c >= 0) {
    constexpr int tt = c >> 1, h = c & 1;
    asm volatile("v_add_f32_dpp %0, v%c1, v%c2 quad_perm:[0,1,2,3] row_mask:%c3 bank_mask:0xf" : "+v"(sc[(tt >> 2) * 2 + h])
                 : "i"(RS + 4 * tt + 2 * h), "i"(RS + 4 * tt + 2 * h + 1), "i"(1 << (tt & 3)));
  }
}
template <int... N> DEV void qk_all(u32x4 (&A)[4], float (&sc)[4], u32 idx, u32 kaddr, std::integer_sequence<int, N...>) {
  (qk_step<N>(A, sc, idx, kaddr), ...);
}
template <int TT, int H> DEV void qk_compact(float (&sc)[4]) {
  asm volatile("v_add_f32_dpp %0, v%c1, v%c2 quad_perm:[0,1,2,3] row_mask:%c3 bank_mask:0xf" : "+v"(sc[(TT >> 2) * 2 + H])
               : "i"(RS + 4 * TT + 2 * H), "i"(RS + 4 * TT + 2 * H + 1), "i"(1 << (TT & 3)));
}
template <int T, int A> DEV void pv_step(u32 idx) {
  constexpr int b = (T * 4 + A) * 8;
  asm volatile("v_smfmac_f32_16x16x64_bf16 v[%c1:%c2], v[%c3:%c4], a[%c5:%c6], %0 abid:1" ::"v"(idx), "i"(RR + 4 * A), "i"(RR + 4 * A + 3),
               "i"(RS + 4 * T), "i"(RS + 4 * T + 3), "i"(b), "i"(b + 7));
}
template <int T> DEV void pv_tile(u32 idx) { pv_step<T, 0>(idx); pv_step<T, 1>(idx); pv_step<T, 2>(idx); pv_step<T, 3>(idx); }

DEV f32x4 vzero4() {
  u64 a, b;
  asm volatile("v_mov_b64 %0, 0" : "=v"(a));
  asm volatile("v_mov_b64 %0, 0" : "=v"(b));
  u32x2 lo = __builtin_bit_cast(u32x2, a), hi = __builtin_bit_cast(u32x2, b);
  u32x4 r = __builtin_shufflevector(lo, hi, 0, 1, 2, 3);
  return __builtin_bit_cast(f32x4, r);
}
DEV void gemv_mm(const u32x4 (&x)[4], u32 idx, f32x4& a0, f32x4& a1) {
  asm volatile("v_smfmac_f32_16x16x64_bf16 %0, %2, v[40:47], %6\n\t"
               "v_smfmac_f32_16x16x64_bf16 %1, %2, v[48:55], %6\n\t"
               "v_smfmac_f32_16x16x64_bf16 %0, %3, v[56:63], %6\n\t"
               "v_smfmac_f32_16x16x64_bf16 %1, %3, v[64:71], %6\n\t"
               "v_smfmac_f32_16x16x64_bf16 %0, %4, v[72:79], %6\n\t"
               "v_smfmac_f32_16x16x64_bf16 %1, %4, v[80:87], %6\n\t"
               "v_smfmac_f32_16x16x64_bf16 %0, %5, v[88:95], %6"
               : "+v"(a0), "+v"(a1) : "v"(x[0]), "v"(x[1]), "v"(x[2]), "v"(x[3]), "v"(idx));
}
DEV float gemv_fin(f32x4 a0, f32x4 a1, u32 lane) {
  asm volatile("s_nop 7" : "+v"(a0), "+v"(a1));
  const bool hi = lane & 32;
  float A = hi ? a1[0] + a1[1] : a0[0] + a0[1];
  float B = hi ? a1[2] + a1[3] : a0[2] + a0[3];
  swap32(A, B);
  return A + B;
}
DEV float colsum(u32 idx) {
  f32x4 a0 = vzero4(), a1 = vzero4();
  u32x4 one = {vconst<0x3F803F80u>(), vconst<0x3F803F80u>(), vconst<0x3F803F80u>(), vconst<0x3F803F80u>()};
  asm volatile("s_nop 1\n\t"
               "v_smfmac_f32_16x16x64_bf16 %0, %2, v[40:47], %3\n\t"
               "v_smfmac_f32_16x16x64_bf16 %1, %2, v[48:55], %3\n\t"
               "v_smfmac_f32_16x16x64_bf16 %0, %2, v[56:63], %3\n\t"
               "v_smfmac_f32_16x16x64_bf16 %1, %2, v[64:71], %3\n\t"
               "v_smfmac_f32_16x16x64_bf16 %0, %2, v[72:79], %3\n\t"
               "v_smfmac_f32_16x16x64_bf16 %1, %2, v[80:87], %3\n\t"
               "v_smfmac_f32_16x16x64_bf16 %0, %2, v[88:95], %3\n\t"
               "s_nop 7"
               : "+v"(a0), "+v"(a1) : "v"(one), "v"(idx));
  return (a0[0] + a0[1]) + (a1[0] + a1[1]);
}
DEV f32x4 ln_stats(const u32x4 (&x)[4], u32 lane, float s1, float s2, float onef, u64& bad) {
  const bf16x2 one = __builtin_bit_cast(bf16x2, 0x3F803F80u);
#pragma unroll
  for (int u = 0; u < 4; u++)
#pragma unroll
    for (int k = 0; k < 4; k++) {
      bf16x2 v = asb(x[u][k]);
      s1 = __builtin_amdgcn_fdot2_f32_bf16(v, one, s1, false);
      s2 = __builtin_amdgcn_fdot2_f32_bf16(v, v, s2, false);
    }
  bad = nan_mask(s1, s1);
  s1 += dppf<QP_X1>(s1);
  s2 += dppf<QP_X1>(s2);
  float z = (lane & 1) ? s2 : s1;
  z += dppf<ROR(8)>(z);
  f32x4 D;
  asm volatile("s_nop 1\n\tv_mfma_f32_16x16x4_f32 %0, %1, %2, 0" : "=&v"(D) : "v"(z), "v"(onef));
  return D;
}
DEV float ln_fin(f32x4 a0, f32x4 a1, f32x4 D, float cs, u32 lane) {
  asm volatile("" : "+v"(D));
  const bool hi = lane & 32;
  float S1 = hi ? D[2] : D[0], S2 = hi ? D[3] : D[1];
  float m = S1 * (1.0f / 448.0f);
  float var = fmaxf(S2 * (1.0f / 448.0f) - m * m, 0.0f);
  float r = __builtin_amdgcn_rsqf(var + 1e-5f), mc = m * cs;
  asm volatile("" : "+v"(r), "+v"(mc));
  return r * (gemv_fin(a0, a1, lane) - mc);
}
#define UNDEF4(x) asm volatile("" : "=v"(x[0]), "=v"(x[1]), "=v"(x[2]), "=v"(x[3]))
template <int O1, int O2, int O3> DEV void ld4(u32x4 (&x)[4], rsrc_t rs, u32 v0, u32 v3, u64 m) {
  asm volatile("s_cmp_eq_u64 %7, 0\n\ts_cbranch_scc1 1f\n\t"
               "s_mov_b64 exec, %7\n\t"
               "buffer_load_dwordx4 %0, %4, %6, 0 offen sc1\n\t"
               "buffer_load_dwordx4 %1, %4, %6, 0 offen offset:%c8 sc1\n\t"
               "buffer_load_dwordx4 %2, %4, %6, 0 offen offset:%c9 sc1\n\t"
               "buffer_load_dwordx4 %3, %5, %6, 0 offen offset:%c10 sc1\n\t"
               "s_mov_b64 exec, -1\n\t"
               "s_waitcnt vmcnt(0)\n1:"
               : "+&v"(x[0]), "+&v"(x[1]), "+&v"(x[2]), "+&v"(x[3])
               : "v"(v0), "v"(v3), "s"(rs), "s"(m), "i"(O1), "i"(O2), "i"(O3) : "memory", "scc");
}
DEV void ld_x(u32x4 (&x)[4], rsrc_t rs, u32 v0, u32 v3, u64 m) { ld4<1024, 2048, 3072>(x, rs, v0, v3, m); }
#define RETRY_IF(c) { if (__builtin_expect(!(c), 1)) break; asm volatile("s_nop 0"); if (--n <= 0) break; }
enum { EV_WO = 0, EV_GU, EV_WQ };
template <int E> DEV void ev_set(int val) {
  asm volatile("v_mov_b32 v%c1, 0\n\tv_mov_b32 v%c2, %0\n\tds_write_b32 v%c1, v%c2 offset:%c3" ::"s"(val), "i"(RT), "i"(RT + 1), "i"(L_EV + E * 4) : "memory");
}
template <int E> DEV void ev_wait(int val) {
  NOUNROLL for (int n = 0; n < 4000000; n++) {
    int v;
    asm volatile("v_mov_b32 v%c1, 0\n\tds_read_b32 v%c1, v%c1 offset:%c2\n\ts_waitcnt lgkmcnt(0)\n\tv_readfirstlane_b32 %0, v%c1"
                 : "=s"(v) : "i"(RT), "i"(L_EV + E * 4) : "memory");
    if (v >= val) break;
  }
}
template <int N> DEV void nap() {
  if constexpr (N > 127) { asm volatile("s_sleep 127"); nap<N - 127>(); }
  else if constexpr (N > 0) asm volatile("s_sleep %c0" ::"i"(N));
}
#define XLANE (g * 256 + (lane & 8) * 16 + (lane & 1) * 64 + ((lane >> 1) & 3) * 16)
#define BQ (2 * (g & 1) + (g >> 1))
DEV u32 xline(u32 nt) { return ((nt >> 3) * 8 + (nt & 3) * 2 + ((nt >> 2) & 1)) * 128; }

template <int T> DEV void wo_mfma(u32x4 B) {
  constexpr int d = RB + 4 * (T & ~3);
  if constexpr ((T & 3) == 0) asm volatile("v_mfma_f32_16x16x32_bf16 v[%c1:%c2], v[%c1:%c2], %0, 0" ::"v"(B), "i"(d), "i"(d + 3));
  else asm volatile("v_mfma_f32_16x16x32_bf16 v[%c1:%c2], v[%c3:%c4], %0, v[%c1:%c2]" ::"v"(B), "i"(d), "i"(d + 3), "i"(RB + 4 * T), "i"(RB + 4 * T + 3));
}
template <int A> DEV void wo_store(u32 voff, rsrc_t rs) {
  constexpr int d = RB + 16 * A;
  asm volatile("v_cvt_pk_bf16_f32 v%c2, v%c2, v%c3\n\tv_cvt_pk_bf16_f32 v%c3, v%c4, v%c5\n\t"
               "buffer_store_dwordx2 v[%c2:%c3], %0, %1, 0 offen offset:%c6 sc1" ::"v"(voff), "s"(rs), "i"(d), "i"(d + 1), "i"(d + 2), "i"(d + 3), "i"(A * 512) : "memory");
}
DEV u32 sel(u32 v, u64 m) { u32 r; asm volatile("v_cndmask_b32 %0, 0, %1, %2" : "=v"(r) : "v"(v), "s"(m)); return r; }

#define CLK(i)
#define STAMP(i)
#define STAMPV(i, val)

DEV int wq_tile(int x, int i) {
  if (WQMAP == 0) return x * 32 + i;
  if (WQMAP == 1) return (4 * (i >> 2) + (x & 3) + 32 * (x >> 2)) * 4 + (i & 3);
  return (4 * (i >> 1) + (x & 3) + 64 * (x >> 2)) * 2 + (i & 1);
}
enum { R_NONE = 0, R_WO = 1, R_WQ = 2, R_GU = 3, R_DN = 4 };
#define LANE_VARS u32 lane; asm volatile("v_mbcnt_lo_u32_b32 %0, -1, 0\n\tv_mbcnt_hi_u32_b32 %0, -1, %0" : "=v"(lane)); \
  const u32 c16 = lane & 15, g = lane >> 4; (void)c16; (void)g; \
  const int w = opq(w_), cu = opq(cu_), xcd = cu >> 5, ix = cu & 31; (void)xcd; (void)ix; \
  const u32 sbase = cu * 262144 + w * 8192, kbm = L_K + w * 24576 - 512, wb = L_W + w * 14336, sb = L_SRC + w * 1536; \
  (void)sbase; (void)kbm; (void)wb; (void)sb;
DEV u64 opq64(u64 x) { asm volatile("" : "+s"(x)); return x; }
DEV int opq(int x) { asm volatile("" : "+s"(x)); return x; }

__global__ __launch_bounds__(256, 1) __attribute__((amdgpu_num_vgpr(38))) void fused_kernel(
    const char* __restrict__ xin, const char* __restrict__ kc, const char* __restrict__ vc, const char* __restrict__ wq,
    const char* __restrict__ wo, const char* __restrict__ wg, const char* __restrict__ wu, const char* __restrict__ wd,
    char* __restrict__ out, char* __restrict__ ws) {
  asm volatile("" ::: "v255", "a255");
  const u32 lane_ = threadIdx.x & 63;
  const int w = __builtin_amdgcn_readfirstlane(threadIdx.x >> 6);
  const int xcd = blockIdx.x & 7, ix = blockIdx.x >> 3;
  const int cu = xcd * 32 + ix, w_ = w, cu_ = cu;
  int pass = 0;
  STAMP(0);
  CLK(14);

  int role_ = R_NONE, rid_ = 0;
  const bool spare = ix >= 28 && xcd == 7;
  if (ix < 28 || spare) {
    if (w == 0) { role_ = R_WO; rid_ = cu; }
    else if (w == 1) { role_ = R_WQ; rid_ = wq_tile(xcd, ix); }
    else if (w == 2) { role_ = R_GU; rid_ = xcd * 28 + (spare ? ix - 28 : ix); }
    else if (xcd == 7) {}
    else if (ix < 4) { role_ = R_WO; rid_ = xcd * 32 + 28 + ix; }
    else if (ix < 8) { role_ = R_WQ; rid_ = wq_tile(xcd, 24 + ix); }
  } else { role_ = R_DN; rid_ = xcd * 4 + ix - 28; }

  const rsrc_t rsK = mk_rs(kc, 0x4000000), rsV = mk_rs(vc, 0x4000000), rsX = mk_rs(xin, 3584);
  const u32 sbase = cu * 262144 + w * 8192;
  const u32 kbm = L_K + w * 24576 - 512, wb = L_W + w * 14336, sb = L_SRC + w * 1536;
  const u32 voffK = (lane_ & 15) * 512 + (lane_ >> 4) * 16;

  auto w_rowg = [&](u32 lane) {
    const u32 rho = lane >> 1, hf = rho >> 4, gb = (rho >> 2) & 3, q = rho & 3;
    return 16 * (gb >> 1) + 8 * (q >> 1) + 4 * (gb & 1) + 2 * hf + (q & 1);
  };
  auto gu_piece = [&](u32 f) {
    LANE_VARS
    dma16(((lane & 1) ? wu : wg) + ((w_rowg(lane) + f * 32) * 3584 + (xcd * 28 + (ix >= 28 ? ix - 28 : ix)) * 16), L_W + 2 * 14336 + f * 1024);
  };
  auto dn_piece = [&](u32 f) {
    LANE_VARS
    dma16(wd + ((w * 448 + f * 32 + w_rowg(lane)) * 896 + opq(rid_) * 32 + (lane & 1) * 16), wb + f * 1024);
  };
  auto extra = [&](int t) {
    LANE_VARS
    const u32 f = w + 4 * (t & 3);
    if (opq(role_) == R_DN) { if (!DN_LATE) dn_piece(t); }
    else if (t < 12 && f < 14) {
      if (t < 4) {
        const char* src = wq + ((w_rowg(lane) + f * 32) * 8192 + (lane & 1) * 16);
        dma16(src + wq_tile(xcd, ix) * 32, L_W + 14336 + f * 1024);
        if (xcd < 7 && (ix >> 2) == 1) dma16(src + wq_tile(xcd, 24 + ix) * 32, L_W + 3 * 14336 + f * 1024);
      } else if (t < 8) {
        const u32 c = f * 64 + lane;
        const char* src = wo + (c * 16 + ((c * 2341) >> 16) * 448 + (cu >> 1) * (32 * 896) + (cu & 1) * 448);
        dma16(src, L_W + f * 1024);
        if (xcd < 7 && ix < 4) dma16(src + 14 * (32 * 896), L_W + 3 * 14336 + f * 1024);
      } else if (!GU_LATE) gu_piece(f);
    }
  };

  if (WQ_FIRST) { extra(0); extra(1); extra(2); extra(3); }
  ld_k<0>(voffK, rsK, sbase, kbm);
  if (threadIdx.x < 16) *LDSP(u32, L_EV + threadIdx.x * 4) = 0;
  u32x4 x[4];
  UNDEF4(x);
  u32 xcur;
  asm volatile("" : "=v"(xcur));
  {
    const rsrc_t rsH = mk_rs(ws, WS_FILL + 256 * FILL_PER_CU);
    u32x2 tg;
    asm volatile("" : "=v"(tg));
    if (w == 3) {
      asm volatile("buffer_load_dwordx2 %0, off, %1, 0 sc1" : "+&v"(tg) : "s"(rsH) : "memory");
      const u32x4 s4 = {SENT, SENT, SENT, SENT};
#pragma unroll
      for (int i = 0; i < 9; i++) __builtin_amdgcn_raw_buffer_store_b128(s4, rsH, WS_FILL + cu * FILL_PER_CU + i * 1024 + lane_ * 16, 0, FILLPOL);
    }
    if (!WQ_FIRST) extra(0);
    if (role_ == R_DN) {
      const u32 g = lane_ >> 4;
      asm volatile("buffer_load_short_d16_hi %0, %1, %2, 0 offen" : "+&v"(xcur) : "v"(BQ * 896 + rid_ * 32 + (lane_ & 15) * 2), "s"(rsX) : "memory");
    }
    if (role_ == R_WQ) {
      const u32 xo0 = ((lane_ >> 1) & 3) * 896 + (lane_ & 8) * 16 + (lane_ >> 4) * 32 + (lane_ & 1) * 16;
      asm volatile("buffer_load_dwordx4 %0, %4, %6, 0 offen\n\t"
                   "buffer_load_dwordx4 %1, %4, %6, 0 offen offset:256\n\t"
                   "buffer_load_dwordx4 %2, %4, %6, 0 offen offset:512\n\t"
                   "buffer_load_dwordx4 %3, %5, %6, 0 offen offset:768"
                   : "+&v"(x[0]), "+&v"(x[1]), "+&v"(x[2]), "+&v"(x[3]) : "v"(xo0), "v"(OOB_IF(xo0, lane_ & 8)), "s"(rsX) : "memory");
    }
    STAMP(1);
    ld_k<1>(voffK, rsK, sbase, kbm);
    STAMP(2);
    if (w == 3) {
      asm volatile("s_waitcnt vmcnt(8)" : "+v"(tg)::"memory");
      const u64 t = (((u64)tg.y << 32) | tg.x) * 6364136223846793005ull + 1442695040888963407ull;
      tg = u32x2{(u32)t, (u32)(t >> 32)};
      __builtin_amdgcn_raw_buffer_store_b64(tg, rsH, OOB_IF(WS_FLAGS + cu * 8, lane_), 0, 16);
    }
    STAMP(3);
    if (!WQ_FIRST) extra(1);
    ld_k<2>(voffK, rsK, sbase, kbm);
    if (!WQ_FIRST) extra(2);
    ld_k<3>(voffK, rsK, sbase, kbm);
    if (!WQ_FIRST) extra(3);
    STAMP(5);
    u32x4 f0, f1;
    asm volatile("" : "=v"(f0), "=v"(f1));
    const u32 fo = WS_FLAGS + lane_ * 16;
    if (w == 3)
      asm volatile("buffer_load_dwordx4 %0, %2, %3, 0 offen sc1\n\t"
                   "buffer_load_dwordx4 %1, %2, %3, 0 offen offset:1024 sc1" : "+&v"(f0), "+&v"(f1) : "v"(fo), "s"(rsH) : "memory");
    ld_k<4>(voffK, rsK, sbase, kbm); extra(4); ld_k<5>(voffK, rsK, sbase, kbm); extra(5);
    STAMP(6);
    asm volatile("s_waitcnt vmcnt(16)" : "+v"(x[0]), "+v"(x[1]), "+v"(x[2]), "+v"(x[3]), "+v"(f0), "+v"(f1), "+v"(xcur)::"memory");
    xcur &= 0xFFFF0000u;
    if (w == 3) {
      STAMP(7);
      NOUNROLL for (int spin = 0;; spin++) {
        STAMPV(19, 30000 - spin);
        u32 d = ((f0.x ^ tg.x) | (f0.y ^ tg.y)) | ((f0.z ^ tg.x) | (f0.w ^ tg.y)) | ((f1.x ^ tg.x) | (f1.y ^ tg.y)) | ((f1.z ^ tg.x) | (f1.w ^ tg.y));
        if (__builtin_amdgcn_ballot_w64(d != 0) == 0 || spin > SPIN_MAX) break;
        __builtin_amdgcn_s_sleep(8);
        asm volatile("buffer_load_dwordx4 %0, %2, %3, 0 offen sc1\n\t"
                     "buffer_load_dwordx4 %1, %2, %3, 0 offen offset:1024 sc1\n\t"
                     "s_waitcnt vmcnt(0)" : "+&v"(f0), "+&v"(f1) : "v"(fo), "s"(rsH) : "memory");
      }
      if (cu == 0) __builtin_amdgcn_raw_buffer_store_b64(tg, rsH, OOB_IF(0u, lane_), 0, 16);
    }
    STAMP(8);
    lds_barrier();
    STAMP(9);
  }

  const u32 idx = ((lane_ & 1) ? 0xEEEEu : 0x4444u) | (((lane_ & 3) * 0x5555u) << 16);

#pragma nounroll
  for (pass = 1; pass < 3; pass++) {
    pass = opq(pass);
    const int role = opq(role_);
    const bool first = pass == 1;
    const rsrc_t rs = mk_rs(ws + WS_FILL + (pass == 2 ? LAYER_SZ : 0), LAYER_SZ);
    STAMP(1);
    if (role == R_WQ) {
      LANE_VARS
      const int rid = opq(rid_);
      w_read(wb + lane * 8);
      float cs = colsum(idx);
      const u32 xo = OFF_XIN + xcd * 4096 + XLANE;
      u32 qo = OFF_Q + (BQ * 8 + (rid >> 5)) * 1024 + ((rid & 3) * 16 + ((rid >> 2) & 3) * 4 + ((rid >> 4) & 1) * 2 + ((lane >> 1) & 1)) * 16 + (c16 >> 2) * 4 + (lane & 1) * 2;
      asm volatile("" : "+v"(cs), "+v"(qo));
      if (!first) { ev_wait<EV_GU>(1); nap<D_XIN>(); }
      u64 m = opq64((u64)(long)(1 - pass));
      f32x4 a0, a1, D;
      STAMP(18);
      NOUNROLL for (int n = (pass >> 1) * SPIN_MAX;;) {
        a0 = vzero4(); a1 = vzero4();
        const float s1 = asf(vconst<0>()), s2 = asf(vconst<0>()), onef = asf(vconst<0x3F800000u>());
        ld_x(x, rs, xo, OOB_IF(xo, lane & 8), m);
        STAMP(2); STAMPV(19, n);
        D = ln_stats(x, lane, s1, s2, onef, m);
        gemv_mm(x, idx, a0, a1);
        RETRY_IF(m != 0);
        nop<7>();
      }
      st16(rs, qo, 0, ln_fin(a0, a1, D, cs, lane) * (0.0625f * 1.4426950408889634f));
      if (w == 1) ev_set<EV_WQ>(pass);
      STAMP(3);
    }
    {
      LANE_VARS
      u32x4 A[4];
      asm volatile("" : "=v"(A[0]));
      const u32 qo = OFF_Q + (cu >> 3) * 1024 + lane * 16;
      const u32 voffV = g * 2048 + c16 * 8;
      const u32 kaddr = kbm + 512 + lane * 16;
      zero_regs<RS, 32>();
      if (first) {
        const u32 voffK = c16 * 512 + g * 16;
        ld_k<6>(voffK, rsK, sbase, kbm); extra(6); ld_k<7>(voffK, rsK, sbase, kbm); extra(7);
        ld_v<0>(voffV, rsV, sbase, w); extra(8); ld_v<1>(voffV, rsV, sbase, w); extra(9);
        ld_v<2>(voffV, rsV, sbase, w); extra(10); extra(12); ld_v<3>(voffV, rsV, sbase, w); extra(11); extra(13);
        asm volatile("buffer_load_dwordx4 %0, %1, %2, 0 offen sc1" : "+&v"(A[0]) : "v"(qo), "s"(rs) : "memory");
        ld_v<4>(voffV, rsV, sbase, w); ld_v<5>(voffV, rsV, sbase, w);
        asm volatile("s_waitcnt vmcnt(32)" : "+v"(A[0])::"memory");
      }
      ds_rd128<RR, 0>(kaddr); ds_rd128<RR + 4, 1024>(kaddr); ds_rd128<RR + 8, 2048>(kaddr);
      ds_rd128<RR + 12, 3072>(kaddr); ds_rd128<RR + 16, 4096>(kaddr); ds_rd128<RR + 20, 5120>(kaddr);
      const u32 kap = 8 * ((c16 >> 1) & 1) + 2 * (c16 >> 2) + (c16 & 1);
      u32 pa = sb + g * 128 + kap * 4, ra = sb + ((lane >> 2) & 1) * 64 + g * 16;
      float sc[4];
      const u32 l32 = (lane & 31) * 16;
      u32 oa = sb + l32, o1 = ((w + 3) & 3) * 1536 + l32, o2 = ((w + 2) & 3) * 1536 + 512 + l32, o3 = ((w + 1) & 3) * 1536 + 1024 + l32;
      u32 sa = L_STAT + (w * 2 + (g & 1)) * 32;
      u32 sw = L_STAT + (((lane & 3) * 2 + (lane >> 5)) * 4 + ((lane - w) & 3)) * 8;
      u32 po = OFF_P + ((((cu >> 3) & 7) * 2 + (g & 1)) * 8 + 2 * w + (c16 >> 3)) * 4096 + ((cu & 7) * 4 + (cu >> 6)) * 128 + (c16 & 1) * 64 + ((c16 >> 1) & 3) * 16;
      po = OOB_IF(po, lane & 32);
      asm volatile("" : "+v"(pa), "+v"(ra), "=v"(sc[0]), "=v"(sc[1]), "=v"(sc[2]), "=v"(sc[3]));
      asm volatile("" : "+v"(oa), "+v"(o1), "+v"(o2), "+v"(o3), "+v"(sa), "+v"(po), "+v"(sw));
      wait_lgkm<0>();
      if (!first) {
        if (role == R_DN) nap<D_Q2>();
        else { ev_wait<EV_WQ>(2); nap<D_Q>(); }
      }
      u64 m = opq64((u64)(long)(1 - pass));
      STAMP(20);
      NOUNROLL for (int n = opq(SPIN_MAX);;) {
        asm volatile("s_cmp_eq_u64 %3, 0\n\ts_cbranch_scc1 1f\n\ts_mov_b64 exec, %3\n\t"
                     "buffer_load_dwordx4 %0, %1, %2, 0 offen sc1\n\ts_mov_b64 exec, -1\n\ts_waitcnt vmcnt(0)\n1:"
                     : "+&v"(A[0]) : "v"(qo), "s"(rs), "s"(m) : "memory", "scc");
        STAMPV(21, n);
        m = nan_mask(asf(A[0].x), asf(A[0].y)) | nan_mask(asf(A[0].z), asf(A[0].w));
        RETRY_IF(m != 0);
      }
      STAMP(4); CLK(14);
      { u32 d; asm volatile("s_nop 1\n\tv_mov_b32_dpp %0, %1 row_ror:4 row_mask:0xf bank_mask:0xf" : "=v"(d) : "v"(A[0][0])); A[1][0] = d; }
      qk_all(A, sc, idx, kaddr, std::make_integer_sequence<int, 32>{});
      nop<7>();
      qk_compact<6, 0>(sc); qk_compact<6, 1>(sc); qk_compact<7, 0>(sc); qk_compact<7, 1>(sc);
      float mx, my;
      asm volatile("v_max_f32 %0, %2, %4\n\tv_max_f32 %1, %3, %5\n\ts_nop 1\n\t"
                   "v_permlane32_swap_b32 %0, %1\n\tv_max_f32 %0, %0, %1\n\tv_mov_b32 %1, %0\n\ts_nop 1\n\t"
                   "v_permlane16_swap_b32 %0, %1\n\tv_max_f32 %0, %0, %1\n\ts_nop 1\n\t"
                   "v_max_f32_dpp %0, %0, %0 row_ror:8 row_mask:0xf bank_mask:0xf\n\ts_nop 1\n\t"
                   "v_max_f32_dpp %0, %0, %0 row_ror:4 row_mask:0xf bank_mask:0xf\n\ts_nop 1\n\t"
                   "v_max_f32_dpp %0, %0, %0 row_ror:2 row_mask:0xf bank_mask:0xf\n\ts_nop 1\n\t"
                   "v_max_f32_dpp %0, %0, %0 row_ror:1 row_mask:0xf bank_mask:0xf\n\ts_nop 0"
                   : "=&v"(mx), "=&v"(my) : "v"(sc[0]), "v"(sc[1]), "v"(sc[2]), "v"(sc[3]));
      const float M0 = asf(__builtin_amdgcn_readlane(asu(mx), 0)), M1 = asf(__builtin_amdgcn_readlane(asu(mx), 32));
      const float p00 = __builtin_amdgcn_exp2f(sc[0] - M0), p01 = __builtin_amdgcn_exp2f(sc[1] - M1);
      const float p10 = __builtin_amdgcn_exp2f(sc[2] - M0), p11 = __builtin_amdgcn_exp2f(sc[3] - M1);
      asm volatile("ds_write2_b32 %0, %1, %2 offset0:0 offset1:16\n\tds_write2_b32 %0, %3, %4 offset0:128 offset1:144" ::"v"(pa),
                   "v"(lo_bf16(p00)), "v"(lo_bf16(p01)), "v"(lo_bf16(p10)), "v"(lo_bf16(p11)) : "memory");
      ds_rd128<RS, 0>(ra); ds_rd128<RS + 4, 128>(ra); ds_rd128<RS + 8, 256>(ra); ds_rd128<RS + 12, 384>(ra);
      ds_rd128<RS + 16, 512>(ra); ds_rd128<RS + 20, 640>(ra); ds_rd128<RS + 24, 768>(ra); ds_rd128<RS + 28, 896>(ra);
      zero_regs<RR, 16>();
      float l0 = p00 + p10, l1 = p01 + p11;
      swap32(l0, l1);
      float lx = l0 + l1, ly = lx;
      swap16(lx, ly);
      lx += ly;
      lx += dppf<ROR(8)>(lx); lx += dppf<ROR(4)>(lx); lx += dppf<ROR(2)>(lx); lx += dppf<ROR(1)>(lx);
      *LDSP(u32x2, sw) = u32x2{asu(mx), asu(lx)};
      if (first) { ld_v<6>(voffV, rsV, sbase, w); ld_v<7>(voffV, rsV, sbase, w); }
      STAMP(5);
      wait_lgkm<0>();
      pv_tile<0>(idx); pv_tile<1>(idx); pv_tile<2>(idx); pv_tile<3>(idx);
      wait_vm<48>(); pv_tile<4>(idx);
      wait_vm<32>(); pv_tile<5>(idx);
      wait_vm<16>(); pv_tile<6>(idx);
      wait_vm<12>(); pv_step<7, 0>(idx);
      wait_vm<8>(); pv_step<7, 1>(idx);
      wait_vm<4>(); pv_step<7, 2>(idx);
      wait_vm<0>(); pv_step<7, 3>(idx);
      nop<7>();
      ds_wr128<RR + 4, 0>(oa); ds_wr128<RR + 8, 512>(oa); ds_wr128<RR + 12, 1024>(oa);
      CLK(15); STAMP(6);
      lds_barrier();
      f32x4 s01 = *LDSP(f32x4, sa), s23 = *LDSP(f32x4, sa + 16);
      f32x4 v1 = *LDSP(f32x4, o1), v2 = *LDSP(f32x4, o2), v3 = *LDSP(f32x4, o3);
      float M;
      asm("v_max3_f32 %0, %1, %2, %3\n\tv_max_f32 %0, %0, %4" : "=&v"(M) : "v"(s01[0]), "v"(s01[2]), "v"(s23[0]), "v"(s23[2]));
      float f0 = __builtin_amdgcn_exp2f(s01[0] - M), f1 = __builtin_amdgcn_exp2f(s01[2] - M);
      float f2 = __builtin_amdgcn_exp2f(s23[0] - M), f3 = __builtin_amdgcn_exp2f(s23[2] - M);
      const float L = __builtin_fmaf(f0, s01[1], f1 * s01[3]) + __builtin_fmaf(f2, s23[1], f3 * s23[3]);
      const float rl = __builtin_amdgcn_rcpf(L);
      const u32 lse = asu(M + __builtin_amdgcn_logf(L));
      float o[4];
      asm volatile("v_mul_f32 %0, %4, v72\n\tv_mul_f32 %1, %4, v73\n\tv_mul_f32 %2, %4, v74\n\tv_mul_f32 %3, %4, v75"
                   : "=&v"(o[0]), "=&v"(o[1]), "=&v"(o[2]), "=&v"(o[3]) : "v"(f0));
      u32x4 pv;
#pragma unroll
      for (int i = 0; i < 4; i++) {
        float t = __builtin_fmaf(f3, v3[i], __builtin_fmaf(f2, v2[i], __builtin_fmaf(f1, v1[i], o[i]))) * rl;
        pv[i] = __builtin_amdgcn_perm(asu(t), lse, 0x07060500u + i);
      }
      st128(rs, po, pv);
      STAMP(7);
    }
    if (role == R_WO) {
      LANE_VARS
      const int rid = opq(rid_);
      w_read<32, 1792>(wb + (8 * g + (c16 >> 2)) * 448 + (lane & 3) * 8);
      const u32 go = OFF_P + (rid >> 1) * 4096 + c16 * 128 + g * 16;
      u32 so = OFF_W + rid * 1792 + c16 * 32 + g * 8, so3 = OOB_IF(so, lane & 8);
      const u64 m0 = opq64(0x000F000F000F000Full), m1 = m0 << 4, m2 = m0 << 8, m3 = m0 << 12;
      asm volatile("" : "+v"(so), "+v"(so3));
      UNDEF4(x);
      float o[8], rd;
      nap<D_WO>();
      u64 m = opq64(~0ull);
      STAMP(24);
      NOUNROLL for (int n = opq(SPIN_MAX);;) {
        ld4<64, 2048, 2112>(x, rs, go, go, m);
        const u32x4 a0 = x[0], a1 = x[1], b0 = x[2], b1 = x[3];
        STAMP(8); STAMPV(25, n);
        const float la = asf(__builtin_amdgcn_perm(__builtin_amdgcn_perm(a0.w, a0.z, 0x0c0c0400u), __builtin_amdgcn_perm(a0.y, a0.x, 0x0c0c0400u), 0x05040100u));
        const float lb = asf(__builtin_amdgcn_perm(__builtin_amdgcn_perm(b0.w, b0.z, 0x0c0c0400u), __builtin_amdgcn_perm(b0.y, b0.x, 0x0c0c0400u), 0x05040100u));
        float M;
        asm("v_max_f32 %0, %1, %2\n\ts_nop 1\n\t"
            "v_max_f32_dpp %0, %0, %0 row_ror:4 row_mask:0xf bank_mask:0xf\n\ts_nop 1\n\t"
            "v_max_f32_dpp %0, %0, %0 row_ror:8 row_mask:0xf bank_mask:0xf" : "=&v"(M) : "v"(la), "v"(lb));
        const float wa = __builtin_amdgcn_exp2f(la - M), wb2 = __builtin_amdgcn_exp2f(lb - M);
        float den = wa + wb2;
        den += dppf<ROR(4)>(den); den += dppf<ROR(8)>(den);
        rd = __builtin_amdgcn_rcpf(den);
#pragma unroll
        for (int i = 0; i < 4; i++) {
          o[i] = __builtin_fmaf(wb2, asf(b0[i]), wa * asf(a0[i]));
          o[4 + i] = __builtin_fmaf(wb2, asf(b1[i]), wa * asf(a1[i]));
        }
        m = nan_mask(o[4], o[5]);
        RETRY_IF(m != 0);
      }
#pragma unroll
      for (int i = 0; i < 8; i++) { o[i] += dppf<ROR(4)>(o[i]); }
#pragma unroll
      for (int i = 0; i < 8; i++) { o[i] += dppf<ROR(8)>(o[i]); o[i] *= rd; }
      const u32 B0 = pk_bf16(o[0], o[1]), B1 = pk_bf16(o[2], o[3]), B2 = pk_bf16(o[4], o[5]), B3 = pk_bf16(o[6], o[7]);
      u32x4 P = {sel(B0, m0), sel(B1, m0), sel(B2, m0), sel(B3, m0)}, Q, R, S;
      Q[0] = sel(B0, m1); Q[1] = sel(B1, m1);
      wo_mfma<0>(P); Q[2] = sel(B2, m1); Q[3] = sel(B3, m1);
      wo_mfma<4>(P); R[0] = sel(B0, m2); R[1] = sel(B1, m2);
      wo_mfma<12>(P); R[2] = sel(B2, m2); R[3] = sel(B3, m2);
      wo_mfma<1>(Q); S[0] = sel(B0, m3); S[1] = sel(B1, m3);
      wo_mfma<13>(Q); S[2] = sel(B2, m3); S[3] = sel(B3, m3);
      wo_mfma<2>(R); wo_mfma<5>(Q); wo_mfma<3>(S); wo_mfma<8>(P); wo_mfma<6>(R);
      wo_store<3>(so3, rs);
      wo_mfma<9>(Q); wo_mfma<7>(S);
      nop<1>();
      wo_store<0>(so, rs);
      wo_mfma<10>(R); wo_mfma<11>(S);
      nop<3>();
      wo_store<1>(so, rs);
      nop<3>();
      wo_store<2>(so, rs);
      if (w == 0) ev_set<EV_WO>(pass);
      STAMP(9);
    }
    if (role == R_GU) {
      if (GU_LATE && first) {
        ev_wait<EV_WO>(pass);
        NOUNROLL for (u32 f = 0; f < 14; f++) gu_piece(f);
        wait_vm<0>();
      }
      LANE_VARS
      const int rid = opq(rid_);
      w_read(wb + lane * 8);
      float cs = colsum(idx);
      const u32 xo = OFF_X1 + xcd * 4096 + XLANE;
      const u32 q8 = rid % 56;
      u32 so = OFF_H + (rid / 56) * 4096 + xline(q8 >> 1) + ((q8 & 1) * 4 + BQ) * 16 + c16 * 2;
      so = OOB_IF(so, lane & 8) | (ix >= 28 ? 1u << 30 : 0u);
      asm volatile("" : "+v"(cs), "+v"(so));
      UNDEF4(x);
      ev_wait<EV_WO>(pass); nap<D_GU>();
      u64 m = opq64(~0ull);
      f32x4 a0, a1, D;
      STAMP(28);
      NOUNROLL for (int n = opq(SPIN_MAX);;) {
        a0 = vzero4(); a1 = vzero4();
        const float s1 = asf(vconst<0>()), s2 = asf(vconst<0>()), onef = asf(vconst<0x3F800000u>());
        ld_x(x, rs, xo, OOB_IF(xo, lane & 8), m);
        STAMP(12); STAMPV(29, n);
        D = ln_stats(x, lane, s1, s2, onef, m);
        gemv_mm(x, idx, a0, a1);
        RETRY_IF(m != 0);
        nop<7>();
      }
      const float y = ln_fin(a0, a1, D, cs, lane);
      const float e = __builtin_amdgcn_exp2f(y * __builtin_fmaf(y * y, -0.044715f * 2.3022081986f, -2.3022081986f));
      st16(rs, so, 0, y * dppf<ROR(8)>(y) * __builtin_amdgcn_rcpf(1.0f + e));
      ev_set<EV_GU>(pass);
      STAMP(13);
    }
    if (role == R_DN) {
      if (DN_LATE && first) { NOUNROLL for (u32 f = 0; f < 14; f++) dn_piece(f); }
      LANE_VARS
      const int rid = opq(rid_);
      u32 so = xline(rid) + ((c16 >> 3) * 4 + BQ) * 16 + (c16 & 7) * 2 + w * 8192;
      u32 la = lane * 4;
      {
        u32 go = OFF_W + (w * 32 + (lane >> 3)) * 3584 + rid * 128 + (lane & 7) * 16;
        const u32 one = 0x3F80u << ((lane & 1) * 16), q = c16 >> 2;
        u32x4 SEL = {q == 0 ? one : 0u, q == 1 ? one : 0u, q == 2 ? one : 0u, q == 3 ? one : 0u};
        const u32 b = (c16 >> 1) & 3;
        u32 wa = sb + ((2 * (b & 1) + (b >> 1)) * 16 + (c16 & 1) * 8 + 2 * g) * 4;
        asm volatile("" : "+v"(so), "+v"(la), "+v"(go), "+v"(SEL), "+v"(wa));
        UNDEF4(x);
        nap<D_RED>();
        u64 m = opq64(~0ull);
        f32x4 D;
        STAMP(26);
        NOUNROLL for (int n = opq(SPIN_MAX);;) {
          asm volatile("s_mov_b64 exec, %7\n\t"
                       "buffer_load_dwordx4 %0, %5, %6, 0 offen sc1\n\t"
                       "buffer_load_dwordx4 %1, %5, %6, %8 offen sc1\n\t"
                       "buffer_load_dwordx4 %2, %5, %6, %9 offen sc1\n\t"
                       "buffer_load_dwordx4 %3, %5, %6, %10 offen sc1\n\t"
                       "s_mov_b64 exec, -1\n\t"
                       "s_waitcnt vmcnt(3)\n\tv_mfma_f32_16x16x32_bf16 %4, %11, %0, 0\n\t"
                       "s_waitcnt vmcnt(2)\n\tv_mfma_f32_16x16x32_bf16 %4, %11, %1, %4\n\t"
                       "s_waitcnt vmcnt(1)\n\tv_mfma_f32_16x16x32_bf16 %4, %11, %2, %4\n\t"
                       "s_waitcnt vmcnt(0)\n\tv_mfma_f32_16x16x32_bf16 %4, %11, %3, %4\n\t"
                       "s_nop 7"
                       : "+&v"(x[0]), "+&v"(x[1]), "+&v"(x[2]), "+&v"(x[3]), "=&v"(D)
                       : "v"(go), "s"(rs), "s"(m), "s"(28672), "s"(57344), "s"(86016), "v"(SEL) : "memory");
          STAMP(10); STAMPV(27, n);
          m = nan_mask(D[0], D[1]);
          RETRY_IF(m != 0);
        }
        *LDSP(u32x2, wa) = u32x2{asu(D[0] + dppf<ROR(8)>(D[0])), asu(D[1] + dppf<ROR(8)>(D[1]))};
        lds_barrier();
        const float y = (*LDSP(float, la) + *LDSP(float, 1536 + la)) + (*LDSP(float, 3072 + la) + *LDSP(float, 4608 + la)) + asf(xcur);
        st16(rs, so, OFF_X1, y); st16(rs, so, OFF_X1 + 4096, y);
        xcur = lo_bf16(y) << 16;
        STAMP(11);
      }
      w_read(wb + lane * 8);
      const u32 xo = OFF_H + w * 4096 + XLANE;
      if (!first) so = OOB_IF(BQ * 896 + rid * 32 + c16 * 2, w);
      const rsrc_t rso = mk_rs(first ? ws + WS_FILL + LAYER_SZ + OFF_XIN : out, first ? 32768 : 3584);
      asm volatile("" : "+v"(so));
      nap<D_H>();
      UNDEF4(x);
      STAMP(28);
      NOUNROLL for (int n = opq(SPIN_MAX);;) {
        f32x4 a0 = vzero4(), a1 = vzero4();
        ld_x(x, rs, xo, OOB_IF(xo, lane & 8), ~0ull);
        STAMP(12); STAMPV(29, n);
        gemv_mm(x, idx, a0, a1);
        const float y = gemv_fin(a0, a1, lane);
        *LDSP(float, sb + 256 + la) = y;
        RETRY_IF(nan_mask(y, y) != 0);
      }
      lds_barrier();
      const float y = (*LDSP(float, 256 + la) + *LDSP(float, 1792 + la)) + (*LDSP(float, 3328 + la) + *LDSP(float, 4864 + la)) + asf(xcur);
      st16(rso, so, 0, y); st16(rso, so, 4096, y);
      xcur = lo_bf16(y) << 16;
      STAMP(13);
    }
    UNDEF4(x);
  }
}

extern "C" void kernel_launch(void* const* d_in, const int* in_sizes, int n_in, void* d_out, int out_size, void* d_ws,
                              size_t ws_size, hipStream_t stream) {
  (void)hipFuncSetAttribute((const void*)fused_kernel, hipFuncAttributeMaxDynamicSharedMemorySize, L_TOTAL);
  fused_kernel<<<256, 256, L_TOTAL, stream>>>((const char*)d_in[0], (const char*)d_in[1], (const char*)d_in[2], (const char*)d_in[3],
                                              (const char*)d_in[4], (const char*)d_in[6], (const char*)d_in[7], (const char*)d_in[8],
                                              (char*)d_out, (char*)d_ws);
}
```

```cpp
#include <hip/hip_runtime.h>
#include <stdint.h>
#include <utility>

typedef unsigned u32;
typedef unsigned long long u64;
typedef u32 u32x2 __attribute__((ext_vector_type(2)));
typedef u32 u32x4 __attribute__((ext_vector_type(4)));
typedef float f32x4 __attribute__((ext_vector_type(4)));
typedef float f32x2 __attribute__((ext_vector_type(2)));
typedef __bf16 bf16x2 __attribute__((ext_vector_type(2)));
typedef __amdgpu_buffer_rsrc_t rsrc_t;
#define LDSP(T, off) ((__attribute__((address_space(3))) T*)(size_t)(off))
#define DEV __device__ __forceinline__
#define NOUNROLL _Pragma("nounroll")

constexpr u32 WS_FLAGS = 0x1000, WS_HEAD = 0x10000, WS_PAGE = 0x200000, WS_PROBE = 0x180000;
constexpr u32 OFF_P = 0;
constexpr u32 OFF_W = 524288;
constexpr u32 OFF_Q = OFF_W + 458752;
constexpr u32 OFF_H = OFF_Q + 32768;
constexpr u32 OFF_X1 = OFF_H + 32768;
constexpr u32 OFF_XIN = OFF_X1 + 32768;
constexpr u32 LAYER_SZ = OFF_XIN + 32768;
constexpr u32 FILL_PER_CU = 9216;
static_assert(LAYER_SZ + 8192 <= 128 * FILL_PER_CU && 128 * FILL_PER_CU <= WS_PROBE, "fill covers a layer");
constexpr u32 SENT = 0xFFFFFFFFu;
#define OOB_IF(off, m) ((off) | ((m) << 24))
constexpr int SPIN_MAX = 30000;
#define D_WO 0
#define D_RED 32
#define D_GU 38
#define D_H 31
#define D_H1 5
#define D_XIN 40
#define D_Q 4
#define D_Q2 30
#define KPOL ""
#define VPOL KPOL
#define WPOL ""
#define GUIN 6
#define GUWO 4
#define G1 2
#define G2 2
#define G3 0
#define PW 15
#define PFI 1
constexpr int PF_LEN = 4544;
#define WIDE 15
constexpr u32 L_SRC = 0, L_STAT = 6144, L_EV = 6400, L_STAMP = 6528, L_W = 8192, L_K = 65536, L_TOTAL = 163840;
constexpr int RT = 38, RB = 40, RS = 40, RR = 72, RK = 96;

constexpr int qk_t(int n) { return n < 16 ? (n & 3) : n < 24 ? 4 + (n & 1) : n >> 2; }
constexpr int qk_rho(int n) { return n < 16 ? (n >> 2) : n < 24 ? (n >> 1) & 3 : n & 3; }
constexpr int qk_J(int n) { return ((qk_t(n) & 3) - qk_rho(n)) & 3; }
constexpr bool qk_lds(int n) { return (n & 7) == 0 || (n & 7) == 3 || (n & 7) == 5; }
constexpr int qk_slot(int n) { int c = 0; for (int i = 0; i < n; i++) c += (qk_lds(i) == qk_lds(n)); return c; }
constexpr int qk_find(int t, int J) { for (int n = 0; n < 32; n++) if (qk_t(n) == t && qk_J(n) == J) return n; return -1; }

template <int N> DEV void wait_vm() { asm volatile("s_waitcnt vmcnt(%c0)" ::"i"(N > 63 ? 63 : N) : "memory"); }
template <int N> DEV void wait_lgkm() { asm volatile("s_waitcnt lgkmcnt(%c0)" ::"i"(N) : "memory"); }
template <int N> DEV void nop() { asm volatile("s_nop %c0" ::"i"(N)); }
DEV void lds_barrier() { asm volatile("s_waitcnt lgkmcnt(0)\n\ts_barrier" ::: "memory"); }

template <int T, int J8> DEV void ld_k1(u32 voff, rsrc_t rs, u32 soff, u32 kb) {
  constexpr int n = qk_find(T, J8 >> 1), sl = qk_slot(n);
  if constexpr (qk_lds(n)) {
    asm volatile("s_add_u32 m0, %3, %c5\n\ts_nop 0\n\tbuffer_load_dwordx4 %0, %1, %2 offen offset:%c4" KPOL " lds" ::"v"(voff), "s"(rs), "s"(soff),
                 "s"(kb), "i"(J8 * 64), "i"(sl * 2048 + (J8 & 1) * 1024 - J8 * 64 + 512) : "memory", "scc");
  } else {
    constexpr int r = RK + sl * 8 + (J8 & 1) * 4;
    asm volatile("buffer_load_dwordx4 v[%c3:%c4], %0, %1, %2 offen offset:%c5" KPOL ::"v"(voff), "s"(rs), "s"(soff), "i"(r), "i"(r + 3), "i"(J8 * 64) : "memory");
  }
}
template <int T, int... J> DEV void ld_k_(u32 voff, rsrc_t rs, u32 sbase, u32 kb, std::integer_sequence<int, J...>) {
  asm volatile("" : "+s"(sbase));
  u32 so = sbase + T * 32768;
  (ld_k1<T, J>(voff, rs, so, kb), ...);
}
template <int T> DEV void ld_k(u32 voff, rsrc_t rs, u32 sbase, u32 kb) { ld_k_<T>(voff, rs, sbase, kb, std::make_integer_sequence<int, 8>{}); }

template <int T, int A> DEV void ld_v4(u32 voff, rsrc_t rs, u32 soff) {
  constexpr int r = (T * 4 + A) * 8;
  asm volatile("buffer_load_dwordx2 a[%c3:%c4], %0, %1, %2 offen" VPOL "\n\t"
               "buffer_load_dwordx2 a[%c5:%c6], %0, %1, %2 offen offset:512" VPOL "\n\t"
               "buffer_load_dwordx2 a[%c7:%c8], %0, %1, %2 offen offset:1024" VPOL "\n\t"
               "buffer_load_dwordx2 a[%c9:%c10], %0, %1, %2 offen offset:1536" VPOL ::"v"(voff), "s"(rs), "s"(soff),
               "i"(r), "i"(r + 1), "i"(r + 2), "i"(r + 3), "i"(r + 4), "i"(r + 5), "i"(r + 6), "i"(r + 7) : "memory");
}
template <int T> DEV void ld_v(u32 voff, rsrc_t rs, u32 sbase, int w) {
  asm volatile("" : "+s"(sbase), "+s"(w));
  u32 so = sbase + T * 32768 - w * 8064;
  ld_v4<T, 0>(voff, rs, so + ((w - 0) & 3) * 8192);
  ld_v4<T, 1>(voff, rs, so + ((w - 1) & 3) * 8192);
  ld_v4<T, 2>(voff, rs, so + ((w - 2) & 3) * 8192);
  ld_v4<T, 3>(voff, rs, so + ((w - 3) & 3) * 8192);
}

template <int R, int OFF> DEV void ds_rd128(u32 addr) {
  asm volatile("ds_read_b128 v[%c1:%c2], %0 offset:%c3" ::"v"(addr), "i"(R), "i"(R + 3), "i"(OFF) : "memory");
}
template <int R, int OFF> DEV void ds_wr128(u32 addr) {
  asm volatile("ds_write_b128 %0, v[%c1:%c2] offset:%c3" ::"v"(addr), "i"(R), "i"(R + 3), "i"(OFF) : "memory");
}
template <int R> DEV void zero2() { asm volatile("v_mov_b64 v[%c0:%c1], 0" ::"i"(R), "i"(R + 1)); }
template <int R, int... I> DEV void zero_(std::integer_sequence<int, I...>) { (zero2<R + 2 * I>(), ...); }
template <int R, int N> DEV void zero_regs() { zero_<R>(std::make_integer_sequence<int, N / 2>{}); }

template <int F, int SF, int SH> DEV void w_rd1(u32 addr) {
  asm volatile("ds_read_b64_tr_b16 v[%c1:%c2], %0 offset:%c5\n\tds_read_b64_tr_b16 v[%c3:%c4], %0 offset:%c6" ::"v"(addr),
               "i"(RB + 4 * F), "i"(RB + 4 * F + 1), "i"(RB + 4 * F + 2), "i"(RB + 4 * F + 3), "i"(F * SF), "i"(F * SF + SH) : "memory");
}
template <int SF, int SH, int... F> DEV void w_rd_(u32 addr, std::integer_sequence<int, F...>) { (w_rd1<F, SF, SH>(addr), ...); }
template <int SF = 1024, int SH = 512> DEV void w_read(u32 addr) { w_rd_<SF, SH>(addr, std::make_integer_sequence<int, 14>{}); wait_lgkm<0>(); }
DEV void dma16(const char* src, u32 lds) {
  asm volatile("s_mov_b32 m0, %1\n\ts_nop 0\n\tglobal_load_lds_dwordx4 %0, off" WPOL ::"v"(src), "s"(lds) : "memory");
}

DEV float asf(u32 x) { return __builtin_bit_cast(float, x); }
DEV u32 asu(float x) { return __builtin_bit_cast(u32, x); }
DEV bf16x2 asb(u32 x) { return __builtin_bit_cast(bf16x2, x); }
template <int CTRL> DEV float dppf(float x) { return __builtin_bit_cast(float, __builtin_amdgcn_update_dpp(0, __builtin_bit_cast(int, x), CTRL, 0xf, 0xf, true)); }
constexpr int ROR(int n) { return 0x120 + n; }
constexpr int QP_X1 = 0xB1;
DEV void swap32(float& a, float& b) {
  auto r = __builtin_amdgcn_permlane32_swap(asu(a), asu(b), false, false);
  a = asf(r[0]); b = asf(r[1]);
}
DEV void swap16(float& a, float& b) {
  auto r = __builtin_amdgcn_permlane16_swap(asu(a), asu(b), false, false);
  a = asf(r[0]); b = asf(r[1]);
}
DEV u32 pk_bf16(float lo, float hi) { u32 r; asm("v_cvt_pk_bf16_f32 %0, %1, %2" : "=v"(r) : "v"(lo), "v"(hi)); return r; }
DEV u32 lo_bf16(float lo) { u32 r; asm("v_cvt_pk_bf16_f32 %0, %1, 0" : "=v"(r) : "v"(lo)); return r; }
template <u32 C> DEV u32 vconst() { u32 r; asm volatile("v_mov_b32 %0, %c1" : "=v"(r) : "i"(C)); return r; }
DEV u64 nan_mask(float a, float b) { return __builtin_amdgcn_ballot_w64(__builtin_isunordered(a, b)); }
DEV u32 max4(u32x4 a) { return max(max(a.x, a.y), max(a.z, a.w)); }
DEV bool any_sent(u32 m) { return __builtin_amdgcn_ballot_w64(m == SENT) != 0; }
DEV u32x4 ld128(rsrc_t rs, u32 voff) { return __builtin_amdgcn_raw_buffer_load_b128(rs, voff, 0, 16); }
DEV u32 ld32(rsrc_t rs, u32 voff) { return __builtin_amdgcn_raw_buffer_load_b32(rs, voff, 0, 16); }
DEV void st32(rsrc_t rs, u32 voff, u32 v) { __builtin_amdgcn_raw_buffer_store_b32(v, rs, voff, 0, 16); }
DEV void st16(rsrc_t rs, u32 voff, u32 soff, float v) {
  asm volatile("buffer_store_short %0, %1, %2, %3 offen sc1" ::"v"(lo_bf16(v)), "v"(voff), "s"(rs), "s"(soff) : "memory");
}
template <int CTRL> DEV u32 dppu(u32 x) { return __builtin_amdgcn_update_dpp(0, x, CTRL, 0xf, 0xf, true); }
constexpr int SHL(int n) { return 0x100 + n; }
template <int S> DEV u32x4 gather8(float y) {
  const u32 p = pk_bf16(y, dppf<0xF5>(y));
  return u32x4{p, dppu<SHL(S)>(p), dppu<SHL(2 * S)>(p), dppu<SHL(3 * S)>(p)};
}
DEV u32 fin4(u32 lr, f32x2 xr) {
  const f32x2 y = ((*LDSP(f32x2, lr) + xr) + *LDSP(f32x2, 1536 + lr)) + (*LDSP(f32x2, 3072 + lr) + *LDSP(f32x2, 4608 + lr));
  return pk_bf16(y[0], y[1]);
}
DEV f32x2 unpk(u32 p) { f32x2 r = {asf(p << 16), asf(p & 0xFFFF0000u)}; asm volatile("" : "+v"(r)); return r; }
DEV void st128s(rsrc_t rs, u32 voff, u32 soff, u32x4 v) { __builtin_amdgcn_raw_buffer_store_b128(v, rs, voff, soff, 16); }
DEV void st128(rsrc_t rs, u32 voff, u32x4 v) { __builtin_amdgcn_raw_buffer_store_b128(v, rs, voff, 0, 16); }
DEV rsrc_t mk_rs(const void* p, u32 n) { return __builtin_amdgcn_make_buffer_rsrc((void*)p, (short)0, (int)n, 0x00020000); }
#define USE4(a, b, c, d) asm volatile("" : "+v"(a), "+v"(b), "+v"(c), "+v"(d)::"memory")

template <int TT, int H> DEV void qk_compact(float (&sc)[4]) {
  asm volatile("v_add_f32_dpp %0, v%c1, v%c2 quad_perm:[0,1,2,3] row_mask:%c3 bank_mask:0xf" : "+v"(sc[(TT >> 2) * 2 + H])
               : "i"(RS + 4 * TT + 2 * H), "i"(RS + 4 * TT + 2 * H + 1), "i"(1 << (TT & 3)));
}
constexpr int qk_c(int n) { return n >= 15 && n < 23 ? n - 15 : n >= 25 && n < 29 ? n - 17 : n >= 30 ? n - 18 : -1; }
constexpr int qk_rd(int n) {
  return qk_lds(n) && qk_slot(n) < 9 ? 2 * qk_slot(n) + 6 : n > 0 && qk_lds(n - 1) && qk_slot(n - 1) < 9 ? 2 * qk_slot(n - 1) + 7 : -1;
}
constexpr int qk_cnt(int n) { int c = 0; for (int i = 0; i < n; i++) c += qk_rd(i) > 2 * qk_slot(n + 1) + 1; return c; }
template <int N> DEV void qk_step(u32x4 (&A)[4], float (&sc)[4], u32 idx, u32 kaddr) {
  constexpr int t = qk_t(N), rho = qk_rho(N), sl = qk_slot(N);
  constexpr int br = qk_lds(N) ? RR + (sl % 3) * 8 : RK + sl * 8;
  asm volatile("v_smfmac_f32_16x16x64_bf16 v[%c2:%c3], %0, v[%c4:%c5], %1" ::"v"(A[rho]), "v"(idx), "i"(RS + 4 * t), "i"(RS + 4 * t + 3), "i"(br), "i"(br + 7));
  if constexpr (N < 31 && qk_lds(N + 1) && qk_slot(N + 1) >= 3) wait_lgkm<qk_cnt(N)>();
  if constexpr (qk_rd(N) >= 0) ds_rd128<RR + (qk_rd(N) % 6) * 4, qk_rd(N) * 1024>(kaddr);
  if constexpr (N + 1 < 12) {
    constexpr int r = 1 + (N + 1) / 4, k = (N + 1) & 3;
    u32 d;
    asm volatile("v_mov_b32_dpp %0, %1 row_ror:%c2 row_mask:0xf bank_mask:0xf" : "=v"(d) : "v"(A[0][k]), "i"(4 * r));
    A[r][k] = d;
  }
  if constexpr (qk_c(N) >= 0) qk_compact<(qk_c(N) >> 1), (qk_c(N) & 1)>(sc);
  if constexpr (N == 29) zero2<RR>();
}
template <int... N> DEV void qk_all(u32x4 (&A)[4], float (&sc)[4], u32 idx, u32 kaddr, std::integer_sequence<int, N...>) {
  (qk_step<N>(A, sc, idx, kaddr), ...);
}

DEV f32x4 vzero4() {
  u64 a, b;
  asm volatile("v_mov_b64 %0, 0" : "=v"(a));
  asm volatile("v_mov_b64 %0, 0" : "=v"(b));
  u32x2 lo = __builtin_bit_cast(u32x2, a), hi = __builtin_bit_cast(u32x2, b);
  u32x4 r = __builtin_shufflevector(lo, hi, 0, 1, 2, 3);
  return __builtin_bit_cast(f32x4, r);
}
#define SM(acc, x, r) "v_smfmac_f32_16x16x64_bf16 " acc ", " x ", v[" r "], %8\n\t"
#define SM_FIN "v_add_f32_dpp %0, %9, %10 quad_perm:[0,1,2,3] row_mask:0x3 bank_mask:0xf\n\t" \
               "v_add_f32_dpp %1, %11, %12 quad_perm:[0,1,2,3] row_mask:0x3 bank_mask:0xf\n\t" \
               "s_nop 1\n\tv_permlane32_swap_b32 %0, %1"
DEV float gemv(u32x4 (&x)[4], u32 idx, f32x4 a0, f32x4 a1) {
  float A, B;
  int d0, d1;
  asm volatile(SM("%2", "%4", "40:47") SM("%3", "%4", "48:55") "s_waitcnt vmcnt(2)\n\t"
               SM("%2", "%5", "56:63") SM("%3", "%5", "64:71") "s_waitcnt vmcnt(1)\n\t"
               SM("%2", "%6", "72:79") SM("%3", "%6", "80:87") "s_waitcnt vmcnt(0)\n\t"
               SM("%2", "%7", "88:95") "s_nop 4"
               : "=s"(d0), "=s"(d1), "+v"(a0), "+v"(a1), "+v"(x[0]), "+v"(x[1]), "+v"(x[2]), "+v"(x[3]) : "v"(idx) : "memory");
  asm volatile("v_add_f32 %0, %5, %6\n\tv_add_f32 %1, %7, %8\n\ts_nop 0\n\t" SM_FIN
               : "=&v"(A), "=&v"(B) : "i"(0), "i"(0), "i"(0), "v"(a1[0]), "v"(a1[1]), "v"(a1[2]), "v"(a1[3]),
                 "v"(a0[0]), "v"(a0[1]), "v"(a0[2]), "v"(a0[3]));
  return A + B;
}
DEV void gemv_ln(u32x4 (&x)[4], u32 idx, f32x4& a0, f32x4& a1, f32x4 D, u64 hi, float& m, float& r) {
  m = D[0]; r = D[1];
  asm volatile(SM("%2", "%4", "40:47") SM("%3", "%4", "48:55") SM("%2", "%5", "56:63")
               "v_cndmask_b32 %0, %0, %9, %11\n\t" SM("%3", "%5", "64:71")
               "v_cndmask_b32 %1, %1, %10, %11\n\t" SM("%2", "%6", "72:79")
               "v_add_f32 %1, 0x3727c5ac, %1\n\t" SM("%3", "%6", "80:87")
               "v_fma_f32 %1, -%0, %0, %1\n\t" SM("%2", "%7", "88:95")
               "v_max_f32 %1, 0x3727c5ac, %1\n\tv_rsq_f32 %1, %1"
               : "+v"(m), "+v"(r), "+v"(a0), "+v"(a1)
               : "v"(x[0]), "v"(x[1]), "v"(x[2]), "v"(x[3]), "v"(idx), "v"(D[2]), "v"(D[3]), "s"(hi));
}
DEV float ln_fin(f32x4 a0, f32x4 a1, float m, float r, float cs, float sc) {
  float A, B;
  asm volatile("v_mul_f32 %2, %4, %2\n\tv_mul_f32 %3, %13, %3\n\tv_add_f32 %0, %5, %6\n\tv_add_f32 %1, %7, %8\n\t"
               "v_mul_f32 %2, %3, %2\n\ts_nop 0\n\t" SM_FIN
               : "=&v"(A), "=&v"(B), "+v"(m), "+v"(r) : "v"(cs), "v"(a1[0]), "v"(a1[1]), "v"(a1[2]), "v"(a1[3]),
                 "v"(a0[0]), "v"(a0[1]), "v"(a0[2]), "v"(a0[3]), "s"(sc));
  return __builtin_fmaf(r, A + B, -m);
}
DEV float colsum(u32 idx) {
  f32x4 a0 = vzero4(), a1 = vzero4();
  u32x4 one = {vconst<0x3F803F80u>(), vconst<0x3F803F80u>(), vconst<0x3F803F80u>(), vconst<0x3F803F80u>()};
  asm volatile("s_nop 1\n\t"
               "v_smfmac_f32_16x16x64_bf16 %0, %2, v[40:47], %3\n\t"
               "v_smfmac_f32_16x16x64_bf16 %1, %2, v[48:55], %3\n\t"
               "v_smfmac_f32_16x16x64_bf16 %0, %2, v[56:63], %3\n\t"
               "v_smfmac_f32_16x16x64_bf16 %1, %2, v[64:71], %3\n\t"
               "v_smfmac_f32_16x16x64_bf16 %0, %2, v[72:79], %3\n\t"
               "v_smfmac_f32_16x16x64_bf16 %1, %2, v[80:87], %3\n\t"
               "v_smfmac_f32_16x16x64_bf16 %0, %2, v[88:95], %3\n\t"
               "s_nop 7"
               : "+v"(a0), "+v"(a1) : "v"(one), "v"(idx));
  return (a0[0] + a0[1]) + (a1[0] + a1[1]);
}
template <int W> DEV f32x4 ln_stats(u32x4 (&x)[4], u32 lane, float s1, float s2, float onef, u64& bad) {
  const bf16x2 one = __builtin_bit_cast(bf16x2, 0x3F803F80u);
#pragma unroll
  for (int u = 0; u < 4; u++)
#pragma unroll
    for (int k = 0; k < 4; k++) {
      if (W == 1 && u > 0 && k == 0) asm volatile("s_waitcnt vmcnt(%c3)" : "+v"(x[u]), "+v"(s1), "+v"(s2) : "i"(3 - u) : "memory");
      if (W == 2 && u > 0 && k == 0)
        asm volatile("s_cmp_eq_u64 %4, 0\n\ts_cbranch_scc1 1f\n\ts_waitcnt vmcnt(%c3)\n1:"
                     : "+v"(x[u]), "+v"(s1), "+v"(s2) : "i"(3 - u), "s"(bad) : "memory", "scc");
      bf16x2 v = asb(x[u][k]);
      s1 = __builtin_amdgcn_fdot2_f32_bf16(v, one, s1, false);
      s2 = __builtin_amdgcn_fdot2_f32_bf16(v, v, s2, false);
    }
  bad = nan_mask(s1, s1);
  s1 += dppf<QP_X1>(s1);
  s2 += dppf<QP_X1>(s2);
  float z = (lane & 1) ? s2 : s1;
  z += dppf<ROR(8)>(z);
  f32x4 D;
  asm volatile("s_nop 1\n\tv_mfma_f32_16x16x4_f32 %0, %1, %2, 0" : "=&v"(D) : "v"(z), "v"(onef));
  return D;
}
#define UNDEF4(x) asm volatile("" : "=v"(x[0]), "=v"(x[1]), "=v"(x[2]), "=v"(x[3]))
template <int O1, int O2, int O3, int W = 0> DEV void ld4(u32x4 (&x)[4], rsrc_t rs, u32 v0, u32 v3, u64 m) {
  asm volatile("s_cmp_eq_u64 %7, 0\n\ts_cbranch_scc1 1f\n\t"
               "s_mov_b64 exec, %7\n\t"
               "buffer_load_dwordx4 %0, %4, %6, 0 offen sc1\n\t"
               "buffer_load_dwordx4 %1, %4, %6, 0 offen offset:%c8 sc1\n\t"
               "buffer_load_dwordx4 %2, %4, %6, 0 offen offset:%c9 sc1\n\t"
               "buffer_load_dwordx4 %3, %5, %6, 0 offen offset:%c10 sc1\n\t"
               "s_mov_b64 exec, -1\n\t"
               "s_waitcnt vmcnt(%c11)\n1:"
               : "+&v"(x[0]), "+&v"(x[1]), "+&v"(x[2]), "+&v"(x[3])
               : "v"(v0), "v"(v3), "s"(rs), "s"(m), "i"(O1), "i"(O2), "i"(O3), "i"(W) : "memory", "scc");
}
template <bool W> DEV void ld_x(u32x4 (&x)[4], rsrc_t rs, u32 v0, u32 v3, u64 m) { ld4<1024, 2048, 3072, W ? 3 : 0>(x, rs, v0, v3, m); }
#define TRY(n0) asm volatile("v_mov_b32 v38, %0" ::"s"(n0)); \
  NOUNROLL do { u64 live; asm volatile("v_add_u32 v38, -1, v38\n\tv_cmp_lt_i32 %0, 0, v38" : "=s"(live));
#define RETRY_IF(mask) if (__builtin_expect(((mask) & live) == 0, 1)) break; } while (1);
enum { EV_WO = 0, EV_GU, EV_WQ, EV_SH = 16 };
template <int E> DEV void ev_set(int val) {
  asm volatile("v_mov_b32 v%c1, 0\n\tv_mov_b32 v%c2, %0\n\tds_write_b32 v%c1, v%c2 offset:%c3" ::"s"(val), "i"(RT), "i"(RT + 1), "i"(L_EV + E * 4) : "memory");
}
template <int E> DEV void ev_wait(int val) {
  NOUNROLL for (int n = 0; n < 4000000; n++) {
    int v;
    asm volatile("v_mov_b32 v%c1, 0\n\tds_read_b32 v%c1, v%c1 offset:%c2\n\ts_waitcnt lgkmcnt(0)\n\tv_readfirstlane_b32 %0, v%c1"
                 : "=s"(v) : "i"(RT), "i"(L_EV + E * 4) : "memory");
    if (v >= val) break;
  }
}
template <int N> DEV void nap() {
  if constexpr (N > 127) { asm volatile("s_sleep 127"); nap<N - 127>(); }
  else if constexpr (N > 0) asm volatile("s_sleep %c0" ::"i"(N));
}
#define XLANE (g * 256 + (lane & 8) * 16 + (lane & 1) * 64 + ((lane >> 1) & 3) * 16)
#define BQ (2 * (g & 1) + (g >> 1))
DEV u32 xline(u32 nt) { return ((nt >> 3) * 8 + (nt & 3) * 2 + ((nt >> 2) & 1)) * 128; }

template <int T> DEV void wo_mfma(u32x4 B) {
  constexpr int d = RB + 4 * (T & ~3);
  if constexpr ((T & 3) == 0) asm volatile("v_mfma_f32_16x16x32_bf16 v[%c1:%c2], v[%c1:%c2], %0, 0" ::"v"(B), "i"(d), "i"(d + 3));
  else asm volatile("v_mfma_f32_16x16x32_bf16 v[%c1:%c2], v[%c3:%c4], %0, v[%c1:%c2]" ::"v"(B), "i"(d), "i"(d + 3), "i"(RB + 4 * T), "i"(RB + 4 * T + 3));
}
constexpr int wo_a0(int F) { return F < 4 ? 0 : F < 8 ? 4 : F < 11 ? 8 : 11; }
constexpr int wo_off(int F) { return ((F < 8 ? 0 : 4) + F - wo_a0(F)) * 64 + ((wo_a0(F) == 4 || wo_a0(F) == 11) ? 16 : 0); }
template <int F> DEV void wo_rd1(u32 addr) {
  asm volatile("ds_read_b64_tr_b16 v[%c1:%c2], %0 offset:%c5\n\tds_read_b64_tr_b16 v[%c3:%c4], %0 offset:%c6" ::"v"(addr),
               "i"(RB + 4 * F), "i"(RB + 4 * F + 1), "i"(RB + 4 * F + 2), "i"(RB + 4 * F + 3), "i"(wo_off(F)), "i"(wo_off(F) + 1792) : "memory");
}
template <int... F> DEV void wo_rd(u32 addr, std::integer_sequence<int, F...>) { (wo_rd1<F>(addr), ...); wait_lgkm<0>(); }
template <int F> DEV void wo_mm(u32x4 B) {
  constexpr int d = RB + 4 * wo_a0(F);
  if constexpr (F == wo_a0(F)) asm volatile("v_mfma_f32_16x16x32_bf16 v[%c1:%c2], v[%c1:%c2], %0, 0" ::"v"(B), "i"(d), "i"(d + 3));
  else asm volatile("v_mfma_f32_16x16x32_bf16 v[%c1:%c2], v[%c3:%c4], %0, v[%c1:%c2]" ::"v"(B), "i"(d), "i"(d + 3), "i"(RB + 4 * F), "i"(RB + 4 * F + 3));
}
template <int D, int S> DEV void wo_cvt() {
  asm volatile("v_cvt_pk_bf16_f32 v%c0, v%c2, v%c3\n\tv_cvt_pk_bf16_f32 v%c1, v%c4, v%c5" ::"i"(D), "i"(D + 1), "i"(S), "i"(S + 1), "i"(S + 2), "i"(S + 3));
}
template <int D> DEV void wo_st(u32 voff, rsrc_t rs) {
  asm volatile("buffer_store_dwordx4 v[%c2:%c3], %0, %1, 0 offen sc1" ::"v"(voff), "s"(rs), "i"(D), "i"(D + 3) : "memory");
}
template <int A> DEV void wo_store(u32 voff, rsrc_t rs) {
  constexpr int d = RB + 16 * A;
  asm volatile("v_cvt_pk_bf16_f32 v%c2, v%c2, v%c3\n\tv_cvt_pk_bf16_f32 v%c3, v%c4, v%c5\n\t"
               "buffer_store_dwordx2 v[%c2:%c3], %0, %1, %6 offen sc1" ::"v"(voff), "s"(rs), "i"(d), "i"(d + 1), "i"(d + 2), "i"(d + 3), "s"(A * 131072) : "memory");
}
DEV u32 sel(u32 v, u64 m) { u32 r; asm volatile("v_cndmask_b32 %0, 0, %1, %2" : "=v"(r) : "v"(v), "s"(m)); return r; }

#define CLK(i)
#define STAMP(i)
#define STAMPV(i, val)

#define TK(i)
#define TKOUT(n)
DEV int wq_pair(int i) { return ((i & 28) == 4 || i >= 28) ? (0xFECB >> (i & 3) * 4) & 15 : (int)(0xDA9876543100020ull >> (i >> 1) * 4 >> (i >= 8 ? 4 : 0)) & 15; }
DEV int wq_tile(int x, int i) {
  return (4 * wq_pair(i) + (x & 3) + 64 * (x >> 2)) * 2 + ((i & 28) == 4 ? 0 : i >= 28 ? 1 : i & 1);
}
enum { R_NONE = 0, R_WO = 1, R_WQ = 2, R_GU = 3, R_DN = 4 };
#define SBASE (((xcd & 3) * 64 + (xcd >> 2) * 32 + ix) * 262144 + w * 8192)
#define EXT (ix < 8 && !((ix & 3) == 3 && (xcd & 2)))
#define GIN (EXT ? (ix < 4 ? G2 : G1) : ((xcd & 2) && (wq_pair(ix) & 3) == 1) ? G3 : GUIN)
#define XBLK ((xcd & 1) * 4096 + (xcd >> 2) * 8192 + (xcd & 2) * 8192)
#define LANE_VARS u32 lane; asm volatile("v_mbcnt_lo_u32_b32 %0, -1, 0\n\tv_mbcnt_hi_u32_b32 %0, -1, %0" : "=v"(lane)); \
  const u32 c16 = lane & 15, g = lane >> 4; (void)c16; (void)g; \
  const int w = opq(w_), cu = opq(cu_), xcd = cu >> 5, ix = cu & 31; (void)xcd; (void)ix; \
  const u32 sbase = SBASE, kbm = L_K + w * 24576 - 512, wb = L_W + w * 14336, sb = L_SRC + w * 1536; \
  (void)sbase; (void)kbm; (void)wb; (void)sb;
DEV u64 opq64(u64 x) { asm volatile("" : "+s"(x)); return x; }
DEV int opq(int x) { asm volatile("" : "+s"(x)); return x; }

__global__ __launch_bounds__(256, 1) __attribute__((amdgpu_num_vgpr(38))) void fused_kernel(
    const char* __restrict__ xin, const char* __restrict__ kc, const char* __restrict__ vc, const char* __restrict__ wq,
    const char* __restrict__ wo, const char* __restrict__ wg, const char* __restrict__ wu, const char* __restrict__ wd,
    char* __restrict__ out, char* __restrict__ ws) {
  asm volatile("" ::: "v255", "a255");
  const u32 lane_ = threadIdx.x & 63;
  const int w = __builtin_amdgcn_readfirstlane(threadIdx.x >> 6);
  const int xcd = blockIdx.x & 7, ix = blockIdx.x >> 3;
  const int cu = xcd * 32 + ix, w_ = w, cu_ = cu;
  int pass = 0;
  STAMP(0);
  CLK(14);

  int role_ = R_NONE, rid_ = 0;
  const bool spare = ix == 31 && (xcd & 2);
  if (ix < 28 || spare) {
    if (w == 0) { role_ = R_WO; rid_ = cu; }
    else if (w == 1) { role_ = R_WQ; rid_ = wq_tile(xcd, ix); }
    else if (w == 2) { role_ = R_GU; rid_ = xcd * 28 + (spare ? ix - 28 : ix); }
    else if (!EXT) {}
    else if (ix < 4) { role_ = R_WO; rid_ = xcd * 32 + 28 + ix; }
    else { role_ = R_WQ; rid_ = wq_tile(xcd, 24 + ix); }
  } else { role_ = R_DN; rid_ = (xcd >> 2) * 14 + (xcd & 3) * 4 - ((xcd & 3) == 3) + ix - 28; }
  char* const pg = (char*)(((u64)ws + WS_HEAD + WS_PAGE - 1) & ~(u64)(WS_PAGE - 1));

  const rsrc_t rsK = mk_rs(kc, 0x4000000), rsV = mk_rs(vc, 0x4000000), rsX = mk_rs(xin, 3584);
  const u32 sbase = SBASE;
  const u32 kbm = L_K + w * 24576 - 512, wb = L_W + w * 14336, sb = L_SRC + w * 1536;
  const u32 voffK = (lane_ & 15) * 512 + (lane_ >> 4) * 16;

  auto w_rowg = [&](u32 lane) {
    const u32 rho = lane >> 1, hf = rho >> 4, gb = (rho >> 2) & 3, q = rho & 3;
    return 16 * (gb >> 1) + 8 * (q >> 1) + 4 * (gb & 1) + 2 * hf + (q & 1);
  };
  auto gu_piece = [&](u32 f) {
    LANE_VARS
    dma16(((lane & 1) ? wu : wg) + ((w_rowg(lane) + f * 32) * 3584 + (xcd * 28 + (ix >= 28 ? ix - 28 : ix)) * 16), L_W + 2 * 14336 + f * 1024);
  };
  auto extra = [&](int t) {
    LANE_VARS
    const u32 f = w + 4 * (t & 3);
    if (opq(role_) != R_DN && f < 14) {
      if (t < 4) {
        const char* src = wq + ((w_rowg(lane) + f * 32) * 8192 + (lane & 1) * 16);
        dma16(src + wq_tile(xcd, ix) * 32, L_W + 14336 + f * 1024);
        if (EXT && ix >= 4) dma16(src + wq_tile(xcd, 24 + ix) * 32, L_W + 3 * 14336 + f * 1024);
      } else if (t < 8) {
        const u32 c = f * 64 + lane;
        const char* src = wo + (c * 16 + ((c * 2341) >> 16) * 448 + (cu >> 1) * (32 * 896) + (cu & 1) * 448);
        dma16(src, L_W + f * 1024);
        if (EXT && ix < 4) dma16(src + 14 * (32 * 896), L_W + 3 * 14336 + f * 1024);
      } else if (f < GIN) gu_piece(f);
    }
  };

  const bool voter = w == 2 && (u32)(ix - 8) < 16;
  const char* const pb = pg + (ix & 2) * (WS_PAGE / 2) + WS_PROBE + (cu >> 5) * 16384 + (cu & 31) * 256;
  u32 j0;
  if (voter) asm volatile("s_load_dword %0, %1, 0x20000" : "=&s"(j0) : "s"(pb) : "memory");
  ld_k<0>(voffK, rsK, sbase, kbm);
  if (threadIdx.x < 16) *LDSP(u32, L_EV + threadIdx.x * 4) = 0;
  u32x4 x[4];
  UNDEF4(x);
  u32 xcur;
  asm volatile("" : "=v"(xcur));
  {
    const rsrc_t rsH = mk_rs(ws, WS_HEAD), rsF = mk_rs(pg + (cu >> 7) * WS_PAGE + (cu & 127) * FILL_PER_CU, FILL_PER_CU);
    u32 vote = 1;
    auto probe = [&]() {
      const u32 c0 = (ix & 1) * 8192;
      u64 t0, t1, t2;
      asm volatile("s_waitcnt lgkmcnt(0)\n\ts_memtime %0\n\ts_waitcnt lgkmcnt(0)\n\t"
                   "s_load_dword %3, %4, %5\n\ts_waitcnt lgkmcnt(0)\n\ts_memtime %1\n\ts_waitcnt lgkmcnt(0)\n\t"
                   "s_load_dword %3, %4, %6\n\ts_waitcnt lgkmcnt(0)\n\ts_memtime %2\n\ts_waitcnt lgkmcnt(0)"
                   : "=&s"(t0), "=&s"(t1), "=&s"(t2), "+&s"(j0) : "s"(pb), "s"(c0), "s"(c0 ^ 8192) : "memory");
      STAMPV(20, (u32)t1 - (u32)t0); STAMPV(21, (u32)t2 - (u32)t1);
      vote = ((((u32)t0 + (u32)t2 - 2 * (u32)t1) >> 31) ^ ix ^ (xcd >> 2)) & 1;
    };
    u32x2 tg;
    asm volatile("" : "=v"(tg));
    if (w == 3 || voter) asm volatile("buffer_load_dwordx2 %0, off, %1, 0 sc1" : "+&v"(tg) : "s"(rsH) : "memory");
    if (w == 3) {
      const u32x4 s4 = {SENT, SENT, SENT, SENT};
#pragma unroll
      for (int i = 0; i < 9; i++) __builtin_amdgcn_raw_buffer_store_b128(s4, rsF, i * 1024 + lane_ * 16, 0, 16);
    }
    extra(0);
    if (role_ == R_DN) {
      asm volatile("buffer_load_dword %0, %1, %2, 0 offen" : "+&v"(xcur) : "v"(((lane_ >> 2) & 3) * 896 + rid_ * 32 + (lane_ & 16) + (lane_ & 3) * 4), "s"(rsX) : "memory");
    }
    if (role_ == R_WQ) {
      const u32 xo0 = ((lane_ >> 1) & 3) * 896 + (lane_ & 8) * 16 + (lane_ >> 4) * 32 + (lane_ & 1) * 16;
      asm volatile("buffer_load_dwordx4 %0, %4, %6, 0 offen\n\t"
                   "buffer_load_dwordx4 %1, %4, %6, 0 offen offset:256\n\t"
                   "buffer_load_dwordx4 %2, %4, %6, 0 offen offset:512\n\t"
                   "buffer_load_dwordx4 %3, %5, %6, 0 offen offset:768"
                   : "+&v"(x[0]), "+&v"(x[1]), "+&v"(x[2]), "+&v"(x[3]) : "v"(xo0), "v"(OOB_IF(xo0, lane_ & 8)), "s"(rsX) : "memory");
    }
    STAMP(1);
    ld_k<1>(voffK, rsK, sbase, kbm);
    STAMP(2);
    if (voter) probe();
    if (w == 3 || voter) {
      asm volatile("s_waitcnt vmcnt(8)" : "+v"(tg)::"memory");
      const u64 t = (((u64)tg.y << 32) | tg.x) * 6364136223846793005ull + 1442695040888963407ull;
      tg = u32x2{((u32)t & ~1u) | vote, (u32)(t >> 32)};
      const u32 fa = voter ? 2048 + (ix & 2) * 4 + (xcd * 8 + ((ix - 8) >> 2) * 2 + (ix & 1)) * 16 : cu * 8;
      __builtin_amdgcn_raw_buffer_store_b64(tg, rsH, OOB_IF(WS_FLAGS + fa, lane_), 0, 16);
    }
    STAMP(3);
    extra(1);
    ld_k<2>(voffK, rsK, sbase, kbm);
    extra(2);
    ld_k<3>(voffK, rsK, sbase, kbm);
    extra(3);
    STAMP(5);
    const u32 fo = WS_FLAGS + lane_ * 16;
    auto rd_flags = [&]() {
      asm volatile("buffer_load_dwordx4 v[40:43], %0, %1, 0 offen sc1\n\t"
                   "buffer_load_dwordx4 v[44:47], %0, %1, 0 offen offset:1024 sc1\n\t"
                   "buffer_load_dwordx4 v[48:51], %0, %1, 0 offen offset:2048 sc1" ::"v"(fo), "s"(rsH) : "memory");
    };
    if (w == 3) rd_flags();
    ld_k<4>(voffK, rsK, sbase, kbm); extra(4); ld_k<5>(voffK, rsK, sbase, kbm); extra(5);
    STAMP(6);
    asm volatile("s_waitcnt vmcnt(16)" : "+v"(x[0]), "+v"(x[1]), "+v"(x[2]), "+v"(x[3]), "+v"(xcur)::"memory");
    if (w == 3) {
      STAMP(7);
      u64 v0, v1;
      NOUNROLL for (int spin = 0;; spin++) {
        STAMPV(19, 30000 - spin);
        u64 bad, t;
        asm volatile("v_cmp_eq_u32 %2, v48, %4\n\tv_cmp_eq_u32 %3, v50, %4\n\t"
                     "v_or_b32 v48, 1, v48\n\tv_or_b32 v50, 1, v50\n\t"
                     "v_cmp_ne_u64 %0, v[40:41], %5\n\tv_cmp_ne_u64 %1, v[42:43], %5\n\ts_or_b64 %0, %0, %1\n\t"
                     "v_cmp_ne_u64 %1, v[44:45], %5\n\ts_or_b64 %0, %0, %1\n\tv_cmp_ne_u64 %1, v[46:47], %5\n\ts_or_b64 %0, %0, %1\n\t"
                     "v_cmp_ne_u64 %1, v[48:49], %5\n\ts_or_b64 %0, %0, %1\n\tv_cmp_ne_u64 %1, v[50:51], %5\n\ts_or_b64 %0, %0, %1"
                     : "=&s"(bad), "=&s"(t), "=&s"(v0), "=&s"(v1) : "v"(tg.x), "v"(tg) : "scc");
        if (bad == 0 || spin > SPIN_MAX) break;
        __builtin_amdgcn_s_sleep(8);
        rd_flags();
        wait_vm<0>();
      }
      if (cu == 0) __builtin_amdgcn_raw_buffer_store_b64(tg, rsH, OOB_IF(0u, lane_), 0, 16);
      const int n0 = __builtin_popcountll(v0), n1 = __builtin_popcountll(v1);
      STAMPV(24, n0); STAMPV(25, n1);
      ev_set<EV_SH>((((32 - n0) >> 31 & 8192) | ((32 - n1) >> 31 & (8192 << 16))));
    }
    STAMP(8);
    lds_barrier();
    STAMP(9);
  }

  const u32 shift = __builtin_amdgcn_readfirstlane(*LDSP(u32, L_EV + EV_SH * 4));
  STAMPV(26, shift);
  const u32 idx = ((lane_ & 1) ? 0xEEEEu : 0x4444u) | (((lane_ & 3) * 0x5555u) << 16);

#pragma nounroll
  for (pass = 1; pass < 3; pass++) {
    pass = opq(pass);
    const int role = opq(role_);
    const bool first = pass == 1;
    const rsrc_t rs = mk_rs(pg + (first ? shift & 0xFFFF : WS_PAGE + (shift >> 16)), LAYER_SZ);
    STAMP(1);
    if (role == R_WQ) {
      LANE_VARS
      const int rid = opq(rid_);
      w_read(wb + lane * 8);
      float cs = colsum(idx);
      const u32 xo = OFF_XIN + XBLK + XLANE;
      u32 qo = OFF_Q + (g >> 1) * 4096 + (g & 1) * 16384 + (rid >> 7) * 8192 + ((rid >> 5) & 3) * 1024 + ((rid & 3) * 16 + ((rid >> 2) & 3) * 4 + ((rid >> 4) & 1) * 2 + ((lane >> 1) & 1)) * 16 + (c16 >> 2) * 4 + (lane & 1) * 2;
      if (WIDE & 1) qo = OOB_IF(qo, lane & 13);
      asm volatile("" : "+v"(cs), "+v"(qo));
      if (!first) { ev_wait<EV_GU>(1); nap<D_XIN>(); }
      u64 m = opq64((u64)(long)(1 - pass));
      f32x4 a0, a1;
      float mu, r;
      const u64 hi = opq64(~0ull << 32);
      STAMP(18);
      TRY((pass >> 1) * SPIN_MAX)
        nop<7>();
        a0 = vzero4(); a1 = vzero4();
        const float s1 = asf(vconst<0>()), s2 = asf(vconst<0>()), onef = asf(vconst<0x3b124925u>());
        ld_x<(PW & 2) != 0>(x, rs, xo, OOB_IF(xo, lane & 8), m);
        STAMP(2);
        const f32x4 D = ln_stats<PW & 2>(x, lane, s1, s2, onef, m);
        gemv_ln(x, idx, a0, a1, D, hi, mu, r);
      RETRY_IF(m)
      st128(rs, qo, gather8<4>(ln_fin(a0, a1, mu, r, cs, 0.0625f * 1.4426950408889634f)));
      if (w == 1) ev_set<EV_WQ>(pass);
      STAMP(3);
    }
    {
      LANE_VARS
      u32x4 A[4];
      asm volatile("" : "=v"(A[0]));
      const u32 qo = OFF_Q + XBLK + (ix >> 3) * 1024 + lane * 16;
      const u32 voffV = g * 2048 + c16 * 8;
      const u32 kaddr = kbm + 512 + lane * 16;
      zero_regs<RS, 32>();
      if (first) {
        const u32 voffK = c16 * 512 + g * 16;
        ld_k<6>(voffK, rsK, sbase, kbm); extra(6); ld_k<7>(voffK, rsK, sbase, kbm); extra(7);
        ld_v<0>(voffV, rsV, sbase, w); extra(8); ld_v<1>(voffV, rsV, sbase, w); extra(9);
        ld_v<2>(voffV, rsV, sbase, w); ld_v<3>(voffV, rsV, sbase, w);
        asm volatile("buffer_load_dwordx4 %0, %1, %2, 0 offen sc1" : "+&v"(A[0]) : "v"(qo), "s"(rs) : "memory");
        ld_v<4>(voffV, rsV, sbase, w); ld_v<5>(voffV, rsV, sbase, w);
        asm volatile("s_waitcnt vmcnt(32)" : "+v"(A[0])::"memory");
      }
      ds_rd128<RR, 0>(kaddr); ds_rd128<RR + 4, 1024>(kaddr); ds_rd128<RR + 8, 2048>(kaddr);
      ds_rd128<RR + 12, 3072>(kaddr); ds_rd128<RR + 16, 4096>(kaddr); ds_rd128<RR + 20, 5120>(kaddr);
      const u32 kap = 8 * ((c16 >> 1) & 1) + 2 * (c16 >> 2) + (c16 & 1);
      const u32 rl = ((lane >> 2) & 1) * 64 + g * 16;
      u32 pa = sb + g * 128 + kap * 4, r0 = sb + rl, r1 = ((w + 3) & 3) * 1536 + rl, r2 = ((w + 2) & 3) * 1536 + rl, r3 = ((w + 1) & 3) * 1536 + rl;
      float sc[4];
      const u32 lst = opq(L_STAT);
      u32 sw = lst + (g & 1) * 32 + w * 8, sa = lst + (g & 1) * 32 + ((w - lane) & 3) * 8;
      u32 po = OFF_P + (((ix >> 3) * 2 + (g & 1)) * 4 + w) * 16384 + (xcd >> 2) * 8192 + (c16 >> 3) * 4096 + ((ix & 7) * 4 + (xcd & 3)) * 128 + (c16 & 1) * 64 + ((c16 >> 1) & 3) * 16;
      po = OOB_IF(po, lane & 32);
      const int k0 = opq(0x07060500), k1 = opq(0x07060501), k2 = opq(0x07060502), k3 = opq(0x07060503);
      asm volatile("" : "+v"(pa), "=v"(sc[0]), "=v"(sc[1]), "=v"(sc[2]), "=v"(sc[3]));
      asm volatile("" : "+v"(r0), "+v"(r1), "+v"(r2), "+v"(r3), "+v"(sa), "+v"(po), "+v"(sw));
      wait_lgkm<0>();
      if (!first) {
        if (role == R_DN) nap<D_Q2>();
        else { ev_wait<EV_WQ>(2); nap<D_Q>(); }
      }
      u64 m = opq64((u64)(long)(1 - pass));
      STAMP(20);
      TRY(SPIN_MAX)
        asm volatile("s_cmp_eq_u64 %3, 0\n\ts_cbranch_scc1 1f\n\ts_mov_b64 exec, %3\n\t"
                     "buffer_load_dwordx4 %0, %1, %2, 0 offen sc1\n\ts_mov_b64 exec, -1\n\ts_waitcnt vmcnt(0)\n1:"
                     : "+&v"(A[0]) : "v"(qo), "s"(rs), "s"(m) : "memory", "scc");

        m = nan_mask(asf(A[0].x), asf(A[0].y)) | nan_mask(asf(A[0].z), asf(A[0].w));
      RETRY_IF(m)
      STAMP(4); CLK(14);
      u64 tk[8]; (void)tk;
      TK(0);
      { u32 d; asm volatile("s_nop 1\n\tv_mov_b32_dpp %0, %1 row_ror:4 row_mask:0xf bank_mask:0xf" : "=v"(d) : "v"(A[0][0])); A[1][0] = d; }
      qk_all(A, sc, idx, kaddr, std::make_integer_sequence<int, 32>{});
      TK(1);
      zero_regs<RR + 2, 14>();
      qk_compact<7, 0>(sc); qk_compact<7, 1>(sc);
      float M0, M1;
      asm volatile("v_max_f32 v38, %2, %4\n\tv_max_f32 v39, %3, %5\n\ts_nop 1\n\t"
                   "v_permlane16_swap_b32 v38, v39\n\tv_max_f32 v38, v38, v39\n\tv_mov_b32 v39, v38\n\ts_nop 1\n\t"
                   "v_permlane32_swap_b32 v38, v39\n\tv_max_f32 v38, v38, v39\n\ts_nop 1\n\t"
                   "v_max_f32_dpp v38, v38, v38 row_ror:8 row_mask:0xf bank_mask:0xf\n\ts_nop 1\n\t"
                   "v_max_f32_dpp v38, v38, v38 row_ror:4 row_mask:0xf bank_mask:0xf\n\ts_nop 1\n\t"
                   "v_max_f32_dpp v38, v38, v38 row_ror:2 row_mask:0xf bank_mask:0xf\n\ts_nop 1\n\t"
                   "v_max_f32_dpp v38, v38, v38 row_ror:1 row_mask:0xf bank_mask:0xf\n\ts_nop 0\n\t"
                   "v_readlane_b32 %0, v38, 0\n\tv_readlane_b32 %1, v38, 16\n\ts_nop 0\n\t"
                   "v_subrev_f32 %2, %0, %2\n\tv_subrev_f32 %3, %1, %3\n\tv_exp_f32 %2, %2\n\tv_exp_f32 %3, %3\n\t"
                   "v_cvt_pk_bf16_f32 v64, %2, 0\n\tv_cvt_pk_bf16_f32 v65, %3, 0\n\t"
                   "ds_write2_b32 %6, v64, v65 offset0:0 offset1:16\n\t"
                   "ds_read_b128 v[40:43], %7\n\tds_read_b128 v[44:47], %7 offset:128\n\t"
                   "v_subrev_f32 %4, %0, %4\n\tv_subrev_f32 %5, %1, %5\n\tv_exp_f32 %4, %4\n\tv_exp_f32 %5, %5\n\t"
                   "v_cvt_pk_bf16_f32 v66, %4, 0\n\tv_cvt_pk_bf16_f32 v67, %5, 0\n\t"
                   "ds_write2_b32 %6, v66, v67 offset0:128 offset1:144\n\t"
                   "ds_read_b128 v[48:51], %7 offset:256\n\tds_read_b128 v[52:55], %7 offset:384\n\t"
                   "ds_read_b128 v[56:59], %7 offset:512\n\tds_read_b128 v[60:63], %7 offset:640"
                   : "=&s"(M0), "=&s"(M1), "+v"(sc[0]), "+v"(sc[1]), "+v"(sc[2]), "+v"(sc[3]) : "v"(pa), "v"(r0) : "memory");
      TK(2);
      if (first) { ld_v<6>(voffV, rsV, sbase, w); ld_v<7>(voffV, rsV, sbase, w); }
      const bool late = first && role >= R_GU;
      auto fetch = [&]() {
        LANE_VARS
        const char* src; u32 st;
        if (role == R_GU) { src = ((lane & 1) ? wu : wg) + (w_rowg(lane) * 3584 + opq(rid_) * 16); st = 32 * 3584; }
        else { src = wd + ((w * 448 + w_rowg(lane)) * 896 + opq(rid_) * 32 + (lane & 1) * 16); st = 32 * 896; }
        u32 f = role == R_GU ? GIN : 0;
        src += f * st;
        const u32 fw = role == R_GU ? 14 - GUWO : 14;
        NOUNROLL for (; f < 14; f++) {
          if (GUWO && f == fw) ev_wait<EV_WO>(1);
          dma16(src, wb + f * 1024); src += st;
        }
      };
      asm volatile(
          "v_add_f32 %0, %0, %2\n\t"
          "v_add_f32 %1, %1, %3\n\t"
          "s_nop 1\n\t"
          "v_permlane16_swap_b32 %0, %1\n\t"
          "v_add_f32 v39, %0, %1\n\t"
          "v_add_f32 %0, %0, %1\n\t"
          "s_waitcnt lgkmcnt(5)\n\t"
          "v_smfmac_f32_16x16x64_bf16 v[72:75], v[40:43], a[0:7], %4 abid:1\n\t"
          "s_waitcnt lgkmcnt(4)\n\t"
          "s_barrier\n\t"
          "ds_read_b128 v[64:67], %6 offset:0\n\t"
          "v_permlane32_swap_b32 v39, %0\n\t"
          "v_smfmac_f32_16x16x64_bf16 v[72:75], v[44:47], a[32:39], %4 abid:1\n\t"
          "s_waitcnt lgkmcnt(3)\n\t"
          "ds_read_b128 v[68:71], %6 offset:128\n\t"
          "ds_read_b128 v[88:91], %6 offset:256\n\t"
          "v_add_f32 v39, v39, %0\n\t"
          "v_smfmac_f32_16x16x64_bf16 v[72:75], v[48:51], a[64:71], %4 abid:1\n\t"
          "ds_read_b128 v[92:95], %6 offset:384\n\t"
          "ds_read_b128 v[40:43], %6 offset:512\n\t"
          "v_add_f32_dpp v39, v39, v39 row_ror:8 row_mask:0xf bank_mask:0xf\n\t"
          "v_smfmac_f32_16x16x64_bf16 v[72:75], v[52:55], a[96:103], %4 abid:1\n\t"
          "s_waitcnt vmcnt(32) lgkmcnt(5)\n\t"
          "ds_read_b128 v[44:47], %6 offset:640\n\t"
          "v_add_f32_dpp v39, v39, v39 row_ror:4 row_mask:0xf bank_mask:0xf\n\t"
          "v_smfmac_f32_16x16x64_bf16 v[72:75], v[56:59], a[128:135], %4 abid:1\n\t"
          "ds_read_b128 v[48:51], %7 offset:0\n\t"
          "ds_read_b128 v[52:55], %7 offset:128\n\t"
          "v_add_f32_dpp v39, v39, v39 row_ror:2 row_mask:0xf bank_mask:0xf\n\t"
          "v_smfmac_f32_16x16x64_bf16 v[72:75], v[60:63], a[160:167], %4 abid:1\n\t"
          "s_waitcnt lgkmcnt(6)\n\t"
          "ds_read_b128 v[56:59], %7 offset:256\n\t"
          "v_add_f32_dpp v39, v39, v39 row_ror:1 row_mask:0xf bank_mask:0xf\n\t"
          "v_smfmac_f32_16x16x64_bf16 v[76:79], v[64:67], a[8:15], %4 abid:1\n\t"
          "ds_write_b64 %9, v[38:39]\n\t"
          "ds_read_b128 v[60:63], %7 offset:384\n\t"
          "v_smfmac_f32_16x16x64_bf16 v[76:79], v[68:71], a[40:47], %4 abid:1\n\t"
          "s_waitcnt lgkmcnt(6)\n\t"
          "ds_read_b128 v[64:67], %7 offset:512\n\t"
          "v_smfmac_f32_16x16x64_bf16 v[76:79], v[88:91], a[72:79], %4 abid:1\n\t"
          "ds_read_b128 v[68:71], %7 offset:640\n\t"
          "ds_read_b128 v[88:91], %8 offset:0\n\t"
          "v_smfmac_f32_16x16x64_bf16 v[76:79], v[92:95], a[104:111], %4 abid:1\n\t"
          "ds_read_b128 v[92:95], %8 offset:128\n\t"
          "v_smfmac_f32_16x16x64_bf16 v[76:79], v[40:43], a[136:143], %4 abid:1\n\t"
          "s_waitcnt lgkmcnt(8)\n\t"
          "ds_read_b128 v[40:43], %8 offset:256\n\t"
          "v_smfmac_f32_16x16x64_bf16 v[76:79], v[44:47], a[168:175], %4 abid:1\n\t"
          "s_waitcnt lgkmcnt(6)\n\t"
          "s_barrier\n\t"
          "ds_read_b64 v[38:39], %10\n\t"
          "v_smfmac_f32_16x16x64_bf16 v[80:83], v[48:51], a[16:23], %4 abid:1\n\t"
          "ds_read_b128 v[44:47], %8 offset:384\n\t"
          "ds_read_b128 v[48:51], %8 offset:512\n\t"
          "v_smfmac_f32_16x16x64_bf16 v[80:83], v[52:55], a[48:55], %4 abid:1\n\t"
          "ds_read_b128 v[52:55], %8 offset:640\n\t"
          "v_smfmac_f32_16x16x64_bf16 v[80:83], v[56:59], a[80:87], %4 abid:1\n\t"
          "s_waitcnt lgkmcnt(7)\n\t"
          "ds_read_b128 v[56:59], %5 offset:768\n\t"
          "v_smfmac_f32_16x16x64_bf16 v[80:83], v[60:63], a[112:119], %4 abid:1\n\t"
          "ds_read_b128 v[60:63], %6 offset:768\n\t"
          "v_smfmac_f32_16x16x64_bf16 v[80:83], v[64:67], a[144:151], %4 abid:1\n\t"
          "ds_read_b128 v[64:67], %7 offset:768\n\t"
          "v_smfmac_f32_16x16x64_bf16 v[80:83], v[68:71], a[176:183], %4 abid:1\n\t"
          "s_waitcnt lgkmcnt(6)\n\t"
          "ds_read_b128 v[68:71], %8 offset:768\n\t"
          "v_max_f32_dpp %0, v38, v38 quad_perm:[1,0,3,2] row_mask:0xf bank_mask:0xf\n\t"
          "v_smfmac_f32_16x16x64_bf16 v[84:87], v[88:91], a[24:31], %4 abid:1\n\t"
          "ds_read_b128 v[88:91], %5 offset:896\n\t"
          "v_max_f32_dpp %0, %0, %0 quad_perm:[2,3,0,1] row_mask:0xf bank_mask:0xf\n\t"
          "v_smfmac_f32_16x16x64_bf16 v[84:87], v[92:95], a[56:63], %4 abid:1\n\t"
          "ds_read_b128 v[92:95], %6 offset:896\n\t"
          "v_sub_f32 %1, v38, %0\n\t"
          "v_smfmac_f32_16x16x64_bf16 v[84:87], v[40:43], a[88:95], %4 abid:1\n\t"
          "s_waitcnt lgkmcnt(6)\n\t"
          "ds_read_b128 v[40:43], %7 offset:896\n\t"
          "v_exp_f32 %1, %1\n\t"
          "v_smfmac_f32_16x16x64_bf16 v[84:87], v[44:47], a[120:127], %4 abid:1\n\t"
          "ds_read_b128 v[44:47], %8 offset:896\n\t"
          "v_mul_f32 %2, %1, v39\n\t"
          "v_smfmac_f32_16x16x64_bf16 v[84:87], v[48:51], a[152:159], %4 abid:1\n\t"
          "s_nop 0\n\t"
          "v_add_f32_dpp %2, %2, %2 quad_perm:[1,0,3,2] row_mask:0xf bank_mask:0xf\n\t"
          "v_smfmac_f32_16x16x64_bf16 v[84:87], v[52:55], a[184:191], %4 abid:1\n\t"
          "s_waitcnt vmcnt(16) lgkmcnt(5)\n\t"
          "v_add_f32_dpp %2, %2, %2 quad_perm:[2,3,0,1] row_mask:0xf bank_mask:0xf\n\t"
          "v_smfmac_f32_16x16x64_bf16 v[72:75], v[56:59], a[192:199], %4 abid:1\n\t"
          "v_rcp_f32 %3, %2\n\t"
          "v_smfmac_f32_16x16x64_bf16 v[76:79], v[60:63], a[200:207], %4 abid:1\n\t"
          "v_log_f32 %2, %2\n\t"
          "v_smfmac_f32_16x16x64_bf16 v[80:83], v[64:67], a[208:215], %4 abid:1\n\t"
          "s_waitcnt lgkmcnt(0)\n\t"
          "v_mul_f32 %1, %1, %3\n\t"
          "v_smfmac_f32_16x16x64_bf16 v[84:87], v[68:71], a[216:223], %4 abid:1\n\t"
          "s_waitcnt vmcnt(12)\n\t"
          "v_add_f32 %0, %0, %2\n\t"
          "v_smfmac_f32_16x16x64_bf16 v[72:75], v[88:91], a[224:231], %4 abid:1\n\t"
          "s_waitcnt vmcnt(8)\n\t"
          "v_smfmac_f32_16x16x64_bf16 v[76:79], v[92:95], a[232:239], %4 abid:1\n\t"
          "s_waitcnt vmcnt(4)\n\t"
          "v_smfmac_f32_16x16x64_bf16 v[80:83], v[40:43], a[240:247], %4 abid:1\n\t"
          "s_waitcnt vmcnt(0)\n\t"
          "v_smfmac_f32_16x16x64_bf16 v[84:87], v[44:47], a[248:255], %4 abid:1\n\t"
          "v_mul_f32_dpp v52, %1, v72 quad_perm:[0,0,0,0] row_mask:0xf bank_mask:0xf\n\t"
          "v_mul_f32_dpp v53, %1, v73 quad_perm:[0,0,0,0] row_mask:0xf bank_mask:0xf\n\t"
          "v_mul_f32_dpp v54, %1, v74 quad_perm:[0,0,0,0] row_mask:0xf bank_mask:0xf\n\t"
          "v_mul_f32_dpp v55, %1, v75 quad_perm:[0,0,0,0] row_mask:0xf bank_mask:0xf\n\t"
          "v_fmac_f32_dpp v52, %1, v76 quad_perm:[1,1,1,1] row_mask:0xf bank_mask:0xf\n\t"
          "v_fmac_f32_dpp v53, %1, v77 quad_perm:[1,1,1,1] row_mask:0xf bank_mask:0xf\n\t"
          "v_fmac_f32_dpp v54, %1, v78 quad_perm:[1,1,1,1] row_mask:0xf bank_mask:0xf\n\t"
          "v_fmac_f32_dpp v55, %1, v79 quad_perm:[1,1,1,1] row_mask:0xf bank_mask:0xf\n\t"
          "v_fmac_f32_dpp v52, %1, v80 quad_perm:[2,2,2,2] row_mask:0xf bank_mask:0xf\n\t"
          "v_fmac_f32_dpp v53, %1, v81 quad_perm:[2,2,2,2] row_mask:0xf bank_mask:0xf\n\t"
          "v_fmac_f32_dpp v54, %1, v82 quad_perm:[2,2,2,2] row_mask:0xf bank_mask:0xf\n\t"
          "v_fmac_f32_dpp v55, %1, v83 quad_perm:[2,2,2,2] row_mask:0xf bank_mask:0xf\n\t"
          "v_fmac_f32_dpp v52, %1, v84 quad_perm:[3,3,3,3] row_mask:0xf bank_mask:0xf\n\t"
          "v_fmac_f32_dpp v53, %1, v85 quad_perm:[3,3,3,3] row_mask:0xf bank_mask:0xf\n\t"
          "v_fmac_f32_dpp v54, %1, v86 quad_perm:[3,3,3,3] row_mask:0xf bank_mask:0xf\n\t"
          "v_fmac_f32_dpp v55, %1, v87 quad_perm:[3,3,3,3] row_mask:0xf bank_mask:0xf\n\t"
          "v_perm_b32 v52, v52, %0, %13\n\t"
          "v_perm_b32 v53, v53, %0, %14\n\t"
          "v_perm_b32 v54, v54, %0, %15\n\t"
          "v_perm_b32 v55, v55, %0, %16\n\t"
          "buffer_store_dwordx4 v[52:55], %11, %12, 0 offen sc1\n\t"
          : "+v"(sc[0]), "+v"(sc[1]), "+v"(sc[2]), "+v"(sc[3])
          : "v"(idx), "v"(r0), "v"(r1), "v"(r2), "v"(r3), "v"(sw), "v"(sa), "v"(po), "s"(rs), "s"(k0), "s"(k1), "s"(k2), "s"(k3) : "memory");
      CLK(15);
      TK(3);
      TKOUT(4);
      STAMP(7);
      if (late) fetch();
      if (PFI && first && role == R_NONE) {
        u64 pc;
        asm volatile("s_getpc_b64 %0" : "=s"(pc));
        const rsrc_t rsC = mk_rs((const char*)pc, PF_LEN);
        asm volatile("buffer_load_dword v39, %0, %1, 0 offen\n\tbuffer_load_dword v39, %2, %1, 0 offen\n\ts_waitcnt vmcnt(0)"
                     ::"v"(lane * 64), "s"(rsC), "v"(lane * 64 + 4096) : "memory");
      }
    }
    if (role == R_WO) {
      LANE_VARS
      const int rid = opq(rid_);
      wo_rd(wb + (8 * g + (c16 >> 2)) * 448 + (lane & 1) * 8 + (lane & 2) * 16, std::make_integer_sequence<int, 14>{});
      const u32 go = OFF_P + ((rid >> 2) & 31) * 16384 + (rid >> 7) * 8192 + (rid & 2) * 2048 + c16 * 128 + g * 16;
      u32 so = OFF_W + (c16 >> 2) * 65536 + (g >> 1) * 32768 + (rid >> 7) * 16384 + (rid & 1) * 8192 + (rid & 126) * 64 + (c16 & 3) * 32 + (g & 1) * 16;
      u32 so3 = OOB_IF(so + 262144, (c16 + 4) & 16);
      const u64 m0 = opq64(0x000F000F000F000Full), m1 = opq64(m0 << 4), m2 = opq64(m0 << 8), m3 = opq64(m0 << 12);
      asm volatile("" : "+v"(so), "+v"(so3));
      UNDEF4(x);
      float o[8], rd;
      nap<D_WO>();
      u64 m = opq64(~0ull);
      STAMP(24);
      TRY(SPIN_MAX)
        ld4<2048, 64, 2112, (PW & 8) ? 2 : 0>(x, rs, go, go, m);
        const u32x4 a0 = x[0], b0 = x[1];
        STAMP(8);
        const float la = asf(__builtin_amdgcn_perm(__builtin_amdgcn_perm(a0.w, a0.z, 0x0c0c0400u), __builtin_amdgcn_perm(a0.y, a0.x, 0x0c0c0400u), 0x05040100u));
        const float lb = asf(__builtin_amdgcn_perm(__builtin_amdgcn_perm(b0.w, b0.z, 0x0c0c0400u), __builtin_amdgcn_perm(b0.y, b0.x, 0x0c0c0400u), 0x05040100u));
        float M;
        asm("v_max_f32 %0, %1, %2\n\ts_nop 1\n\t"
            "v_max_f32_dpp %0, %0, %0 row_ror:4 row_mask:0xf bank_mask:0xf\n\ts_nop 1\n\t"
            "v_max_f32_dpp %0, %0, %0 row_ror:8 row_mask:0xf bank_mask:0xf" : "=&v"(M) : "v"(la), "v"(lb));
        float wa = __builtin_amdgcn_exp2f(la - M), wb2 = __builtin_amdgcn_exp2f(lb - M);
        asm volatile("s_waitcnt vmcnt(0)" : "+v"(x[2]), "+v"(x[3]), "+v"(wa), "+v"(wb2)::"memory");
        const u32x4 a1 = x[2], b1 = x[3];
        float den = wa + wb2;
        rd = den + dppf<ROR(4)>(den);
#pragma unroll
        for (int i = 0; i < 4; i += 2) {
          const f32x2 wa2 = {wa, wa}, wbb = {wb2, wb2};
          f32x2 t = __builtin_elementwise_fma(wbb, f32x2{asf(b0[i]), asf(b0[i + 1])}, wa2 * f32x2{asf(a0[i]), asf(a0[i + 1])});
          f32x2 u = __builtin_elementwise_fma(wbb, f32x2{asf(b1[i]), asf(b1[i + 1])}, wa2 * f32x2{asf(a1[i]), asf(a1[i + 1])});
          asm("" : "+v"(t)); asm("" : "+v"(u));
          o[i] = t[0]; o[i + 1] = t[1]; o[4 + i] = u[0]; o[5 + i] = u[1];
        }
        m = nan_mask(o[4], o[5]);
      RETRY_IF(m)
#define RA(i, n) "v_add_f32_dpp %" #i ", %" #i ", %" #i " row_ror:" #n " row_mask:0xf bank_mask:0xf\n\t"
      asm volatile(RA(4, 4) RA(5, 4) RA(8, 8) RA(0, 4) RA(1, 4) "v_rcp_f32 %8, %8\n\t" RA(2, 4) RA(3, 4) RA(6, 4) RA(7, 4)
                   RA(0, 8) RA(1, 8) RA(2, 8) RA(3, 8) RA(4, 8) RA(5, 8) RA(6, 8) RA(7, 8)
                   : "+v"(o[0]), "+v"(o[1]), "+v"(o[2]), "+v"(o[3]), "+v"(o[4]), "+v"(o[5]), "+v"(o[6]), "+v"(o[7]), "+v"(rd));
#pragma unroll
      for (int i = 0; i < 8; i += 2) {
        f32x2 t = {o[i], o[i + 1]};
        asm("" : "+v"(t));
        t *= f32x2{rd, rd};
        o[i] = t[0]; o[i + 1] = t[1];
      }
      const u32 B0 = pk_bf16(o[0], o[1]), B1 = pk_bf16(o[2], o[3]), B2 = pk_bf16(o[4], o[5]), B3 = pk_bf16(o[6], o[7]);
      u32x4 P = {sel(B0, m0), sel(B1, m0), sel(B2, m0), sel(B3, m0)}, Q, R, S;
      Q[0] = sel(B0, m1); Q[1] = sel(B1, m1);
      wo_mm<8>(P); Q[2] = sel(B2, m1); Q[3] = sel(B3, m1);
      wo_mm<11>(P); R[0] = sel(B0, m2); R[1] = sel(B1, m2);
      wo_mm<9>(Q); R[2] = sel(B2, m2); R[3] = sel(B3, m2);
      wo_mm<12>(Q); S[0] = sel(B0, m3); S[1] = sel(B1, m3);
      wo_mm<10>(R); S[2] = sel(B2, m3); S[3] = sel(B3, m3);
      wo_mm<13>(R); wo_mm<0>(P); wo_mm<1>(Q); wo_mm<2>(R); wo_mm<3>(S);
      wo_mm<4>(P); nop<1>(); wo_cvt<RB + 32, RB + 32>();
      wo_mm<5>(Q); wo_cvt<RB + 34, RB + 44>();
      wo_mm<6>(R); wo_st<RB + 32>(so3, rs);
      wo_mm<7>(S);
      nop<1>(); wo_cvt<RB, RB>(); nop<3>(); wo_cvt<RB + 2, RB + 16>();
      wo_st<RB>(so, rs);
      if (w == 0) ev_set<EV_WO>(pass);
      STAMP(9);
    }
    if (role == R_GU) {
      LANE_VARS
      const int rid = opq(rid_);
      wait_vm<0>();
      STAMP(16);
      w_read(wb + lane * 8);
      float cs = colsum(idx);
      const u32 xo = OFF_X1 + XBLK + XLANE;
      const u32 q8 = rid % 56;
      u32 so = OFF_H + (xcd & 2) * 2048 + (xcd >> 2) * 16384 + xline(q8 >> 1) + ((q8 & 1) * 4 + BQ) * 16 + c16 * 2;
      so = OOB_IF(so, lane & ((WIDE & 2) ? 15 : 8)) | (ix >= 28 ? 1u << 30 : 0u);
      asm volatile("" : "+v"(cs), "+v"(so));
      UNDEF4(x);
      if (!GUWO || !first) { ev_wait<EV_WO>(pass); nap<D_GU>(); }
      u64 m = opq64(~0ull);
      f32x4 a0, a1;
      float mu, r;
      const u64 hi = opq64(~0ull << 32);
      STAMP(28);
      TRY(SPIN_MAX)
        nop<7>();
        a0 = vzero4(); a1 = vzero4();
        const float s1 = asf(vconst<0>()), s2 = asf(vconst<0>()), onef = asf(vconst<0x3b124925u>());
        ld_x<(PW & 1) != 0>(x, rs, xo, OOB_IF(xo, lane & 8), m);
        STAMP(12);
        const f32x4 D = ln_stats<PW & 1>(x, lane, s1, s2, onef, m);
        gemv_ln(x, idx, a0, a1, D, hi, mu, r);
      RETRY_IF(m)
      const float y = ln_fin(a0, a1, mu, r, cs, 1.0f);
      const float e = __builtin_amdgcn_exp2f(y * __builtin_fmaf(y * y, -0.044715f * 2.3022081986f, -2.3022081986f));
      const float hv = y * dppf<ROR(8)>(y) * __builtin_amdgcn_rcpf(1.0f + e);
      if (WIDE & 2) { const u32x4 h8 = gather8<2>(hv); st128s(rs, so, 0, h8); st128s(rs, so, 8192, h8); }
      else { st16(rs, so, 0, hv); st16(rs, so, 8192, hv); }
      ev_set<EV_GU>(pass);
      STAMP(13);
    }
    if (role == R_DN) {
      u32 so, lr;
      {
        LANE_VARS
        const int rid = opq(rid_);
        so = xline(rid) + (lane & 31) * 4 + (lane & 32) * 128 + w * 8192;
        lr = (lane & 4) * 32 + (lane & 8) * 8 + (lane & 16) * 2 + (lane & 3) * 8;
        u32 go = OFF_W + (rid - 14 * (xcd >> 2)) * 32768 + (w >> 1) * 16384 + (xcd >> 2) * 8192 + (w & 1) * 4096 + lane * 16;
        const u32 one = 0x3F80u << ((lane & 1) * 16), q = c16 >> 2;
        u32x4 SEL = {q == 0 ? one : 0u, q == 1 ? one : 0u, q == 2 ? one : 0u, q == 3 ? one : 0u};
        const u32 b = (c16 >> 1) & 3;
        u32 wa = sb + ((2 * (b & 1) + (b >> 1)) * 16 + ((WIDE & 8) ? (c16 & 1) * 4 + (g & 1) * 2 + (g >> 1) * 8 : (c16 & 1) * 8 + 2 * g)) * 4;
        const f32x2 xr = unpk(xcur);
        asm volatile("" : "+v"(so), "+v"(lr), "+v"(go), "+v"(SEL), "+v"(wa));
        UNDEF4(x);
        nap<D_RED>();
        u64 m = opq64(~0ull);
        f32x4 D;
        STAMP(26);
        TRY(SPIN_MAX)
          asm volatile("s_mov_b64 exec, %7\n\t"
                       "buffer_load_dwordx4 %0, %5, %6, 0 offen sc1\n\t"
                       "buffer_load_dwordx4 %1, %5, %6, %8 offen sc1\n\t"
                       "buffer_load_dwordx4 %2, %5, %6, %9 offen sc1\n\t"
                       "buffer_load_dwordx4 %3, %5, %6, %10 offen sc1\n\t"
                       "s_mov_b64 exec, -1\n\t"
                       "s_waitcnt vmcnt(3)\n\tv_mfma_f32_16x16x32_bf16 %4, %11, %0, 0\n\t"
                       "s_waitcnt vmcnt(2)\n\tv_mfma_f32_16x16x32_bf16 %4, %11, %1, %4\n\t"
                       "s_waitcnt vmcnt(1)\n\tv_mfma_f32_16x16x32_bf16 %4, %11, %2, %4\n\t"
                       "s_waitcnt vmcnt(0)\n\tv_mfma_f32_16x16x32_bf16 %4, %11, %3, %4\n\t"
                       "s_nop 7"
                       : "+&v"(x[0]), "+&v"(x[1]), "+&v"(x[2]), "+&v"(x[3]), "=&v"(D)
                       : "v"(go), "s"(rs), "s"(m), "s"(1024), "s"(2048), "s"(3072), "v"(SEL) : "memory");
          STAMP(10);
          float e0, e1;
          asm volatile("v_add_f32_dpp %0, %2, %2 row_ror:8 row_mask:0xf bank_mask:0xf\n\t"
                       "v_add_f32_dpp %1, %3, %3 row_ror:8 row_mask:0xf bank_mask:0xf" : "=&v"(e0), "=&v"(e1) : "v"(D[0]), "v"(D[1]));
          *LDSP(u32x2, wa) = u32x2{asu(e0), asu(e1)};
          asm volatile("" : "+v"(D)::"memory");
          m = nan_mask(D[0], D[1]);
        RETRY_IF(m)
        lds_barrier();
        xcur = fin4(lr, xr);
        __builtin_amdgcn_raw_buffer_store_b32(xcur, rs, so, OFF_X1, 16);
        STAMP(11);
      }
      LANE_VARS
      const int rid = opq(rid_);
      w_read(wb + lane * 8);
      const u32 xo = OFF_H + (w & 1) * 4096 + (xcd >> 2) * 8192 + (w >> 1) * 16384 + XLANE;
      if (!first) so = OOB_IF(((lane >> 2) & 3) * 896 + rid * 32 + (lane & 16) + (lane & 3) * 4, (lane & 32) | w);
      lr += 256;
      u32 la = sb + 256 + lane * 4;
      const f32x2 xr = unpk(xcur);
      const rsrc_t rso = mk_rs(first ? pg + WS_PAGE + (shift >> 16) + OFF_XIN : out, first ? 32768 : 3584);
      asm volatile("" : "+v"(so), "+v"(lr), "+v"(la));
      nap<D_H>();
      if (first) nap<D_H1>();
      UNDEF4(x);
      STAMP(28);
      u64 m = opq64(~0ull);
      u64 bad = __builtin_amdgcn_ballot_w64(vconst<0>() != 0);
      TRY(SPIN_MAX)
        if (bad != 0) {
          float s1 = asf(vconst<0>()), s2 = asf(vconst<0>());
          const bf16x2 one = __builtin_bit_cast(bf16x2, 0x3F803F80u);
#pragma unroll
          for (int k = 0; k < 8; k++) {
            s1 = __builtin_amdgcn_fdot2_f32_bf16(asb(x[k >> 2][k & 3]), one, s1, false);
            s2 = __builtin_amdgcn_fdot2_f32_bf16(asb(x[2 + (k >> 2)][k & 3]), one, s2, false);
          }
          m = nan_mask(s1, s2);
        }
        f32x4 a0 = vzero4(), a1 = vzero4();
        ld_x<true>(x, rs, xo, OOB_IF(xo, lane & 8), m);
        STAMP(12);
        const float y = gemv(x, idx, a0, a1);
        *LDSP(float, la) = y;
        bad = nan_mask(y, y);
      RETRY_IF(bad)
      lds_barrier();
      xcur = fin4(lr, xr);
      st32(rso, so, xcur);
      STAMP(13);
    }
    UNDEF4(x);
  }
}

extern "C" void kernel_launch(void* const* d_in, const int* in_sizes, int n_in, void* d_out, int out_size, void* d_ws,
                              size_t ws_size, hipStream_t stream) {
  (void)hipFuncSetAttribute((const void*)fused_kernel, hipFuncAttributeMaxDynamicSharedMemorySize, L_TOTAL);
  fused_kernel<<<256, 256, L_TOTAL, stream>>>((const char*)d_in[0], (const char*)d_in[1], (const char*)d_in[2], (const char*)d_in[3],
                                              (const char*)d_in[4], (const char*)d_in[6], (const char*)d_in[7], (const char*)d_in[8],
                                              (char*)d_out, (char*)d_ws);
}
```
